```python
import torch

def _softmax(x, axis=-1):
    return torch.softmax(x, dim=axis)
def _gelu(x):
    return torch.nn.functional.gelu(x, approximate="tanh")
def _silu(x):
    return torch.nn.functional.silu(x)
def _sigmoid(x):
    return torch.sigmoid(x)
def _layernorm(x, axis=-1, eps=1e-5):
    m = x.mean(dim=axis, keepdim=True)
    v = x.var(dim=axis, keepdim=True, unbiased=False)
    return (x - m) / torch.sqrt(v + eps)
def _scatter_add(tgt, idx, src, axis=0):
    return tgt.clone().index_add_(axis, idx, src)
def _segment_sum(src, seg, n):
    out = torch.zeros((n,) + src.shape[1:], dtype=src.dtype)
    return out.index_add_(0, seg, src)
def _pad(x, pw):
    flat = tuple(v for pair in reversed(pw) for v in pair)
    return torch.nn.functional.pad(x, flat)

kernel_name = 'sequential_preLN__ssm_conv_s4__mixer_s_mlp__prompt'

def setup_inputs():

    _w = (lambda a: (lambda t: t.to(torch.bfloat16) if t.is_floating_point() else t)(torch.as_tensor(a)))
    def _A_setup():
        B, L, D, N = 1, 2048, 640, 192
        A_re = -(lambda _x: torch.exp(torch.as_tensor(_x)))((lambda *s: torch.randn(*s, dtype=torch.bfloat16) * 6.4)(D, N))
        A_im = torch.pi * (lambda *s: torch.randn(*s, dtype=torch.bfloat16) * 0.05)(D, N)
        B_re = (lambda *s: torch.randn(*s, dtype=torch.bfloat16) * 0.05)(D, N)
        B_im = (lambda *s: torch.randn(*s, dtype=torch.bfloat16) * 0.05)(D, N)
        C_re = (lambda *s: torch.randn(*s, dtype=torch.bfloat16) * 0.05)(D, N)
        C_im = (lambda *s: torch.randn(*s, dtype=torch.bfloat16) * 0.05)(D, N)
        log_dt = (lambda *s: torch.randn(*s, dtype=torch.bfloat16) * 0.05)(D) * 0.5 - 4.0
        Dskip = (lambda *s: torch.randn(*s, dtype=torch.bfloat16) * 0.05)(D)
        return {
            "u": _w((lambda *s: torch.randn(*s, dtype=torch.bfloat16) * 0.05)(B, L, D)),
            "A_re": _w(A_re),
            "A_im": _w(A_im),
            "B_re": _w(B_re),
            "B_im": _w(B_im),
            "C_re": _w(C_re),
            "C_im": _w(C_im),
            "log_dt": _w(log_dt),
            "Dskip": _w(Dskip),
        }
    _A_in = _A_setup()

    def _B_setup():
        B, S, D, F = 1, 2048, 640, 1536
        return {
            "x":  _w((lambda *s: torch.randn(*s, dtype=torch.bfloat16) * 0.05)(B, S, D)),
            "w1": _w((lambda *s: torch.randn(*s, dtype=torch.bfloat16) * 6.4)(D, D) * (D ** -0.5)),
            "w2": _w((lambda *s: torch.randn(*s, dtype=torch.bfloat16) * 6.4)(D, D) * (D ** -0.5)),
            "w3": _w((lambda *s: torch.randn(*s, dtype=torch.bfloat16) * 6.4)(D, F) * (D ** -0.5)),
            "w4": _w((lambda *s: torch.randn(*s, dtype=torch.bfloat16) * 6.4)(F, D) * (F ** -0.5)),
        }
    _B_in = _B_setup()

    return {
        "A_u": _A_in["u"],
        "A_A_re": _A_in["A_re"],
        "A_A_im": _A_in["A_im"],
        "A_B_re": _A_in["B_re"],
        "A_B_im": _A_in["B_im"],
        "A_C_re": _A_in["C_re"],
        "A_C_im": _A_in["C_im"],
        "A_log_dt": _A_in["log_dt"],
        "A_Dskip": _A_in["Dskip"],
        "B_x": _B_in["x"],
        "B_w1": _B_in["w1"],
        "B_w2": _B_in["w2"],
        "B_w3": _B_in["w3"],
        "B_w4": _B_in["w4"],
    }

def reference(A_u, A_A_re, A_A_im, A_B_re, A_B_im, A_C_re, A_C_im, A_log_dt, A_Dskip, B_x, B_w1, B_w2, B_w3, B_w4):
    def _A_ref(u, A_re, A_im, B_re, B_im, C_re, C_im, log_dt, Dskip):
        Bsz, L, D = u.shape
        dt = torch.exp(log_dt)[:, None]
        A = A_re + 1j * A_im
        Bc = B_re + 1j * B_im
        Cc = C_re + 1j * C_im
        logdA = dt * A
        dB = dt * Bc
        l = (lambda x, dt: x.to(dt))(torch.arange(L), A_re.dtype)
        P = torch.exp(logdA[:, :, None] * l[None, None, :])
        K = torch.einsum("dn,dnl,dn->dl", Cc, P, dB).real
        L2 = 2 * L
        Kp = _pad(K, ((0, 0), (0, L)))
        up = (lambda x, *d: x.permute(*d))(u, 0, 2, 1)
        up = _pad(up, ((0, 0), (0, 0), (0, L)))
        Kf = (lambda x, axes=(-2,-1): torch.fft.rfft2(x.float(), dim=axes))(Kp[:, None, :], axes=(-2, -1))
        Uf = (lambda x, axes=(-2,-1): torch.fft.rfft2(x.float(), dim=axes))(up[:, :, None, :], axes=(-2, -1))
        Yf = Uf * Kf[None, :, :, :]
        y = (lambda x, s, axes=(-2,-1): torch.fft.irfft2(x, s=s, dim=axes).to(torch.bfloat16))(Yf, (1, L2), axes=(-2, -1))[:, :, 0, :L]
        y = (lambda x, *d: x.permute(*d))(y, 0, 2, 1) + u * Dskip[None, None, :]
        return y

    def _B_ref(x, w1, w2, w3, w4):
        B, S, D = x.shape
        h = _gelu(_layernorm(x, axis=-1) @ w1)
        c = D // 4
        g0 = _pad(h[:, :, 0*c:1*c], ((0, 0), (1, 0), (0, 0)))[:, :S, :]
        g1 = _pad(h[:, :, 1*c:2*c], ((0, 0), (0, 1), (0, 0)))[:, 1:, :]
        g2 = _pad(h[:, :, 2*c:3*c], ((0, 0), (2, 0), (0, 0)))[:, :S, :]
        g3 = _pad(h[:, :, 3*c:   ], ((0, 0), (0, 2), (0, 0)))[:, 2:, :]
        hs = torch.concatenate([g0, g1, g2, g3], axis=-1) @ w2
        x1 = x + hs
        h2 = _gelu(_layernorm(x1, axis=-1) @ w3) @ w4
        return x1 + h2

    x = A_u
    h = x + _A_ref(_layernorm(x, axis=-1), A_A_re, A_A_im, A_B_re, A_B_im, A_C_re, A_C_im, A_log_dt, A_Dskip)
    h2 = h + _B_ref(_layernorm(h, axis=-1), B_w1, B_w2, B_w3, B_w4)
    return h2
```

```cpp
#include <hip/hip_runtime.h>
#include <hip/hip_bf16.h>
#include <stdint.h>

typedef unsigned short u16;
__device__ __forceinline__ float bf2f(u16 v){ return __uint_as_float(((unsigned)v)<<16); }
__device__ __forceinline__ u16 f2bf(float f){ __hip_bfloat16 b = __float2bfloat16(f); return *reinterpret_cast<u16*>(&b); }
__device__ __forceinline__ float rbf(float f){ return bf2f(f2bf(f)); }

constexpr int L=2048, D=640, N=192, F=1536;

__global__ void k_ln(const u16* x, u16* y){
  int t = blockIdx.x; int lane = threadIdx.x;
  float v[10]; float s=0;
  for(int i=0;i<10;i++){ v[i]=bf2f(x[t*D+lane+64*i]); s+=v[i]; }
  for(int m=32;m>=1;m>>=1) s+=__shfl_xor(s,m);
  float mean=s/D; float q=0;
  for(int i=0;i<10;i++){ float d=v[i]-mean; q+=d*d; }
  for(int m=32;m>=1;m>>=1) q+=__shfl_xor(q,m);
  float var=rbf(q/D); float mb=rbf(mean);
  float sd=rbf(sqrtf(rbf(var+1e-5f)));
  for(int i=0;i<10;i++){ y[t*D+lane+64*i]=f2bf(rbf(v[i]-mb)/sd); }
}
__global__ void k_kgen(const u16* Are,const u16* Aim,const u16* Bre,const u16* Bim,const u16* Cre,const u16* Cim,const u16* logdt,float* K){
  int d=blockIdx.y; int l=blockIdx.x*256+threadIdx.x;
  float dt=rbf(expf(bf2f(logdt[d])));
  float lf=rbf((float)l);
  float acc=0;
  for(int n=0;n<N;n++){
    int i=d*N+n;
    float ar=dt*bf2f(Are[i]), ai=dt*bf2f(Aim[i]);
    float br=dt*bf2f(Bre[i]), bi=dt*bf2f(Bim[i]);
    float cr=bf2f(Cre[i]), ci=bf2f(Cim[i]);
    float wr=cr*br-ci*bi, wi=cr*bi+ci*br;
    float e=expf(ar*lf); float s,c; sincosf(ai*lf,&s,&c);
    acc+= e*(wr*c - wi*s);
  }
  K[d*L+l]=acc;
}
__global__ void k_conv(const u16* x,const u16* u,const float* K,const u16* Dsk,u16* h){
  int t=blockIdx.x; int d=blockIdx.y*64+threadIdx.x;
  float acc=0;
  for(int s=0;s<=t;s++) acc+=K[d*L+t-s]*bf2f(u[s*D+d]);
  float y=rbf(acc);
  float ud=rbf(bf2f(u[t*D+d])*bf2f(Dsk[d]));
  y=rbf(y+ud);
  h[t*D+d]=f2bf(bf2f(x[t*D+d])+y);
}
__device__ float gelu(float x){ return 0.5f*x*(1.f+tanhf(0.7978845608028654f*(x+0.044715f*x*x*x))); }
__global__ void k_gemm(const u16* A,const u16* W,u16* C,int Kd,int Nd,int mode,const u16* r1,const u16* r2){
  int t=blockIdx.x; int n=blockIdx.y*64+threadIdx.x;
  float acc=0;
  for(int k=0;k<Kd;k++){
    int tt=t;
    if(mode==1){ int g=k/160; tt = g==0? t-1 : g==1? t+1 : g==2? t-2 : t+2; }
    float a = (tt>=0 && tt<L)? bf2f(A[tt*Kd+k]) : 0.f;
    acc+=a*bf2f(W[k*Nd+n]);
  }
  acc=rbf(acc);
  if(mode==0) C[t*Nd+n]=f2bf(gelu(acc));
  else if(mode==1) C[t*Nd+n]=f2bf(bf2f(r1[t*Nd+n])+acc);
  else { float v=rbf(bf2f(r1[t*Nd+n])+acc); C[t*Nd+n]=f2bf(bf2f(r2[t*Nd+n])+v); }
}

extern "C" void kernel_launch(void* const* d_in, const int* in_sizes, int n_in,
                              void* d_out, int out_size, void* d_ws, size_t ws_size,
                              hipStream_t stream) {
  const u16* x=(const u16*)d_in[0];
  char* ws=(char*)d_ws;
  u16* u=(u16*)(ws); u16* h=(u16*)(ws+(4<<20)); u16* xp=(u16*)(ws+(8<<20)); u16* xpp=(u16*)(ws+(12<<20));
  u16* h1=(u16*)(ws+(16<<20)); u16* x1=(u16*)(ws+(20<<20)); u16* x1n=(u16*)(ws+(24<<20)); u16* hid=(u16*)(ws+(28<<20));
  float* K=(float*)(ws+(40<<20));
  k_ln<<<L,64,0,stream>>>(x,u);
  k_kgen<<<dim3(L/256,D),256,0,stream>>>((const u16*)d_in[1],(const u16*)d_in[2],(const u16*)d_in[3],(const u16*)d_in[4],(const u16*)d_in[5],(const u16*)d_in[6],(const u16*)d_in[7],K);
  k_conv<<<dim3(L,D/64),64,0,stream>>>(x,u,K,(const u16*)d_in[8],h);
  k_ln<<<L,64,0,stream>>>(h,xp);
  k_ln<<<L,64,0,stream>>>(xp,xpp);
  k_gemm<<<dim3(L,D/64),64,0,stream>>>(xpp,(const u16*)d_in[10],h1,D,D,0,nullptr,nullptr);
  k_gemm<<<dim3(L,D/64),64,0,stream>>>(h1,(const u16*)d_in[11],x1,D,D,1,xp,nullptr);
  k_ln<<<L,64,0,stream>>>(x1,x1n);
  k_gemm<<<dim3(L,F/64),64,0,stream>>>(x1n,(const u16*)d_in[12],hid,D,F,0,nullptr,nullptr);
  k_gemm<<<dim3(L,D/64),64,0,stream>>>(hid,(const u16*)d_in[13],(u16*)d_out,F,D,2,x1,h);
}
```

```cpp
#include <hip/hip_runtime.h>
#include <hip/hip_bf16.h>
#include <stdint.h>

#define GAS __attribute__((address_space(1)))
#define LAS __attribute__((address_space(3)))
typedef unsigned short u16;
typedef unsigned v4u __attribute__((ext_vector_type(4)));
typedef unsigned v2u __attribute__((ext_vector_type(2)));
typedef float f32x4 __attribute__((ext_vector_type(4)));
typedef float f32x2 __attribute__((ext_vector_type(2)));
typedef short bf16x8 __attribute__((ext_vector_type(8)));
typedef short s16x4 __attribute__((ext_vector_type(4)));
typedef __bf16 bf16x2 __attribute__((ext_vector_type(2)));

constexpr int L = 2048, D = 640, NM = 192, F = 1536;
constexpr float EPS = 1e-5f;

constexpr int OFF_WT1 = 65536;
constexpr int OFF_WT2 = OFF_WT1 + 800 * 1024;
constexpr int OFF_WT3 = OFF_WT2 + 800 * 1024;
constexpr int OFF_WT4 = OFF_WT3 + 1920 * 1024;
constexpr int OFF_CS1 = OFF_WT4 + 1920 * 1024;
constexpr int OFF_CS3 = OFF_CS1 + 4096;
constexpr int OFF_ST1 = OFF_CS3 + 8192;
constexpr int OFF_UE = OFF_ST1 + 16384;
constexpr int OFF_H = OFF_UE + 160 * 256 * 128;
constexpr int OFF_H1 = OFF_H + 32 * 160 * 512;
constexpr int OFF_X1 = OFF_H1 + 32 * 20 * 4096;
constexpr int OFF_HID = OFF_X1 + 32 * 20 * 4096;
constexpr int WS_BYTES = OFF_HID + 32 * 48 * 4096;

struct Params {
  const u16 *x, *Are, *Aim, *Bre, *Bim, *Cre, *Cim, *logdt, *Dsk, *w1, *w2, *w3, *w4;
  u16 *out;
  unsigned char *ws;
};

__device__ __forceinline__ float bflo(unsigned v) { return __builtin_bit_cast(float, v << 16); }
__device__ __forceinline__ float bfhi(unsigned v) { return __builtin_bit_cast(float, v & 0xffff0000u); }
__device__ __forceinline__ unsigned pk2(float a, float b) {
  return __builtin_bit_cast(unsigned, __builtin_convertvector(f32x2{a, b}, bf16x2));
}
__device__ __forceinline__ unsigned pk2(f32x2 a) { return __builtin_bit_cast(unsigned, __builtin_convertvector(a, bf16x2)); }
__device__ __forceinline__ f32x4 mfma(v4u a, v4u b, f32x4 c) {
  return __builtin_amdgcn_mfma_f32_16x16x32_bf16(__builtin_bit_cast(bf16x8, a), __builtin_bit_cast(bf16x8, b), c, 0, 0, 0);
}
__device__ __forceinline__ float dot2(unsigned a, unsigned b, float c) {
  return __builtin_amdgcn_fdot2_f32_bf16(__builtin_bit_cast(bf16x2, a), __builtin_bit_cast(bf16x2, b), c, false);
}
__device__ __forceinline__ f32x2 cmul(f32x2 x, f32x2 y) {
  f32x2 t = f32x2{x.x, x.x} * y;
  return __builtin_elementwise_fma(f32x2{x.y, x.y}, f32x2{-y.y, y.x}, t);
}
template <int CTRL> __device__ __forceinline__ float dpp_add(float x) {
  return x + __builtin_bit_cast(float, __builtin_amdgcn_update_dpp(0, __builtin_bit_cast(int, x), CTRL, 0xf, 0xf, false));
}
__device__ __forceinline__ float sum16(float x) {
  x = dpp_add<0x128>(x); x = dpp_add<0x124>(x); x = dpp_add<0x122>(x); x = dpp_add<0x121>(x);
  return x;
}
__device__ __forceinline__ float sum_x16(float x) {
  unsigned a = __builtin_bit_cast(unsigned, x), b = a;

  auto s = __builtin_amdgcn_permlane16_swap(a, b, false, false);
  unsigned s0 = s[0], s1 = s[1];
  return __builtin_bit_cast(float, s0) + __builtin_bit_cast(float, s1);
}
__device__ __forceinline__ float sum_x32(float x) {
  unsigned a = __builtin_bit_cast(unsigned, x), b = a;

  auto s = __builtin_amdgcn_permlane32_swap(a, b, false, false);
  unsigned s0 = s[0], s1 = s[1];
  return __builtin_bit_cast(float, s0) + __builtin_bit_cast(float, s1);
}
__device__ __forceinline__ v2u tr_read(LAS unsigned char *p) {
  return __builtin_bit_cast(v2u, __builtin_amdgcn_ds_read_tr16_b64_v4i16((LAS s16x4 *)p));
}
__device__ __forceinline__ void dma4(const unsigned char *gsrc, unsigned ldst) {
  unsigned keep;
  asm volatile(
      "s_mov_b32 %0, m0\n\ts_mov_b32 m0, %2\n\ts_nop 0\n\t"
      "global_load_lds_dwordx4 %1, off\n\t"
      "global_load_lds_dwordx4 %1, off offset:1024\n\t"
      "global_load_lds_dwordx4 %1, off offset:2048\n\t"
      "global_load_lds_dwordx4 %1, off offset:3072\n\t"
      "s_mov_b32 m0, %0"
      : "=&s"(keep) : "v"(gsrc), "s"(ldst) : "memory");
}
__device__ __forceinline__ void dma1(const unsigned char *gsrc, unsigned ldst) {
  unsigned keep;
  asm volatile("s_mov_b32 %0, m0\n\ts_mov_b32 m0, %2\n\ts_nop 0\n\tglobal_load_lds_dwordx4 %1, off\n\ts_mov_b32 m0, %0"
               : "=&s"(keep) : "v"(gsrc), "s"(ldst) : "memory");
}
template <int N> __device__ __forceinline__ void dma_run(const unsigned char *gsrc, unsigned ldst) {
#pragma unroll
  for (int i = 0; i + 4 <= N; i += 4) dma4(gsrc + i * 1024, ldst + i * 1024);
#pragma unroll
  for (int i = N & ~3; i < N; ++i) dma1(gsrc + i * 1024, ldst + i * 1024);
}
#define WAIT_VM(n) asm volatile("s_waitcnt vmcnt(" #n ")" ::: "memory")
#define BARRIER() asm volatile("s_waitcnt lgkmcnt(0)\n\ts_barrier" ::: "memory")
#define SCHED() __builtin_amdgcn_sched_barrier(0)
__device__ __forceinline__ unsigned lds_addr(LAS unsigned char *p) { return (unsigned)(size_t)p; }

__device__ __forceinline__ f32x2 gelu2(f32x2 x) {
  constexpr float c0 = -2.0f * 0.7978845608028654f * 1.4426950408889634f, c1 = c0 * 0.044715f;
  f32x2 t = __builtin_elementwise_fma(x * x, f32x2{c1, c1}, f32x2{c0, c0}) * x;
  f32x2 e = f32x2{__builtin_amdgcn_exp2f(t.x), __builtin_amdgcn_exp2f(t.y)} + f32x2{1.f, 1.f};
  return x * f32x2{__builtin_amdgcn_rcpf(e.x), __builtin_amdgcn_rcpf(e.y)};
}

__device__ __forceinline__ void phase_ln(const Params &p, LAS unsigned char *lds, int theta) {
  const int tid = threadIdx.x, lane = tid & 63, w = __builtin_amdgcn_readfirstlane(tid >> 6);
  const int tok = tid >> 5, l32 = tid & 31;
  constexpr int PITCH = 1280 + 16;
  const GAS v2u *xr = (const GAS v2u *)(p.x + (size_t)(theta * 8 + tok) * D);
  v2u xv[5], dv[5];
#pragma unroll
  for (int i = 0; i < 5; ++i) xv[i] = xr[l32 + 32 * i];
#pragma unroll
  for (int i = 0; i < 5; ++i) dv[i] = ((const GAS v2u *)p.Dsk)[l32 + 32 * i];
  float s = 0.f, q = 0.f;
#pragma unroll
  for (int i = 0; i < 5; ++i) {
    s = dot2(xv[i].x, 0x3f803f80u, s); s = dot2(xv[i].y, 0x3f803f80u, s);
    q = dot2(xv[i].x, xv[i].x, q); q = dot2(xv[i].y, xv[i].y, q);
  }
  s = sum_x16(sum16(s)); q = sum_x16(sum16(q));
  const float mean = s * (1.f / D);
  const float var = fmaxf(q * (1.f / D) - mean * mean, 0.f);
  const float rstd = __builtin_amdgcn_rsqf(var + EPS);
  const f32x2 rs2 = {rstd, rstd}, mb2 = {-mean * rstd, -mean * rstd};
#pragma unroll
  for (int i = 0; i < 5; ++i) {
    f32x2 x0 = {bflo(xv[i].x), bfhi(xv[i].x)}, x1 = {bflo(xv[i].y), bfhi(xv[i].y)};
    v2u u = {pk2(__builtin_elementwise_fma(x0, rs2, mb2)), pk2(__builtin_elementwise_fma(x1, rs2, mb2))};
    f32x2 u0 = {bflo(u.x), bfhi(u.x)}, u1 = {bflo(u.y), bfhi(u.y)};
    f32x2 d0 = {bflo(dv[i].x), bfhi(dv[i].x)}, d1 = {bflo(dv[i].y), bfhi(dv[i].y)};
    v2u e = {pk2(__builtin_elementwise_fma(u0, d0, x0)), pk2(__builtin_elementwise_fma(u1, d1, x1))};
    *(LAS v2u *)(lds + tok * PITCH + (l32 + 32 * i) * 8) = u;
    *(LAS v2u *)(lds + (8 + tok) * PITCH + (l32 + 32 * i) * 8) = e;
  }
  BARRIER();
  const int G = lane >> 4, qq = (lane >> 2) & 3, pp = lane & 3;
  const int li = lane & 15;
#pragma unroll
  for (int it = 0; it < 5; ++it) {
    const int cb = (w * 5 + it) * 8 + 2 * G;
    LAS unsigned char *src = lds + ((pp & 1) * 8 + qq) * PITCH + (cb + (pp >> 1)) * 8;
    v2u a = tr_read(src), b = tr_read(src + 4 * PITCH);
    const int c4 = cb + (li >> 3);
    const int slot = ((li & 7) + (theta >> 1)) & 7;
    *(GAS v4u *)(p.ws + OFF_UE + (c4 * 256 + theta) * 128 + slot * 16) = v4u{a.x, a.y, b.x, b.y};
  }
}

__device__ __forceinline__ void phase_retile(const Params &p, LAS unsigned char *lds, int task) {
  const int tid = threadIdx.x, lane = tid & 63, w = __builtin_amdgcn_readfirstlane(tid >> 6);
  const u16 *W; int N, strip, ks0, nks, KS, dst, per; int cs = -1;
  if (task < 10) { W = p.w1; N = D; strip = task; ks0 = 0; nks = 5; KS = 20; dst = OFF_WT1; per = 5; cs = OFF_CS1; }
  else if (task < 20) { W = p.w2; N = D; strip = task - 10; ks0 = 0; nks = 5; KS = 20; dst = OFF_WT2; per = 5; }
  else if (task < 44) { W = p.w3; N = F; strip = task - 20; ks0 = 0; nks = 5; KS = 20; dst = OFF_WT3; per = 6; cs = OFF_CS3; }
  else { W = p.w4; N = D; strip = (task - 44) % 10; ks0 = ((task - 44) / 10) * 16; nks = 4; KS = 48; dst = OFF_WT4; per = 5; }
  ks0 += w * nks;
  LAS unsigned char *my = lds + w * 20480;
  {
    const int r8 = lane >> 3, cpos = lane & 7, csrc = cpos ^ (2 * ((r8 >> 1) & 3));
    const unsigned char *src = (const unsigned char *)(W + (size_t)(ks0 * 32 + r8) * N + strip * 64) + csrc * 16;
    for (int i = 0; i < nks * 4; ++i) dma1(src + (size_t)i * 8 * N * 2, lds_addr(my) + i * 1024);
  }
  WAIT_VM(0);
  const int g = lane >> 4, qq = (lane >> 2) & 3, pp = lane & 3;
  f32x4 csum[4];
#pragma unroll
  for (int t = 0; t < 4; ++t) csum[t] = f32x4{0.f, 0.f, 0.f, 0.f};
  const v4u ones = {0x3f803f80u, 0x3f803f80u, 0x3f803f80u, 0x3f803f80u};
  const int row = 4 * g + qq;
  for (int s = 0; s < nks; ++s) {
#pragma unroll
    for (int t = 0; t < 4; ++t) {
      const int chunk = (2 * t + (pp >> 1)) ^ (2 * ((row >> 1) & 3));
      LAS unsigned char *src = my + (s * 32 + row) * 128 + chunk * 16 + (pp & 1) * 8;
      v2u a = tr_read(src), b = tr_read(src + 16 * 128);
      v4u fr = {a.x, a.y, b.x, b.y};
      const int nt = strip * 4 + t;
      const int j = nt / per, rem = nt - j * per;
      const int i = (per == 5 && (j & 1)) ? (rem == 0 ? 4 : rem - 1) : rem;
      *(GAS v4u *)(p.ws + dst + ((j * KS + ks0 + s) * per + i) * 1024 + lane * 16) = fr;
      csum[t] = mfma(fr, ones, csum[t]);
    }
  }
  if (cs >= 0) {
    BARRIER();
    if ((lane & 15) == 0) {
#pragma unroll
      for (int t = 0; t < 4; ++t) *(LAS f32x4 *)(lds + (w * 64 + t * 16 + 4 * g) * 4) = csum[t];
    }
    BARRIER();
    if (tid < 64) {
      LAS float *f = (LAS float *)lds;
      *(GAS float *)(p.ws + cs + (strip * 64 + tid) * 4) = (f[tid] + f[64 + tid]) + (f[128 + tid] + f[192 + tid]);
    }
  }
}

constexpr int CV_UE = 0, CV_KC = 36864, CV_KCW = 16640, CV_KP = 4160, CV_CMP = CV_KC + 4 * CV_KCW, CV_CMPW = 1568;
constexpr int CV_BYTES = CV_CMP + 4 * CV_CMPW;
__device__ __forceinline__ f32x2 cexp_l(float ar2, float af, float l) {
  const float m = __builtin_amdgcn_exp2f(ar2 * l), ph = __builtin_amdgcn_fractf(af * l);
  return f32x2{m * __builtin_amdgcn_cosf(ph), m * __builtin_amdgcn_sinf(ph)};
}
__device__ __forceinline__ void phase_kgen(const Params &p, LAS unsigned char *lds, int c4) {
  const int tid = threadIdx.x, lane = tid & 63, w = __builtin_amdgcn_readfirstlane(tid >> 6);
  const int d = c4 * 4 + w;
  LAS unsigned char *kc = lds + CV_KC + w * CV_KCW;
  LAS unsigned char *cmp = lds + CV_CMP + w * CV_CMPW + 16;
  f32x4 pm0, pm1, pm2;
  {
    unsigned pa[3][6];
#pragma unroll
    for (int r = 0; r < 3; ++r) {
      const int o = d * NM + r * 64 + lane;
      pa[r][0] = p.Are[o]; pa[r][1] = p.Aim[o]; pa[r][2] = p.Bre[o]; pa[r][3] = p.Bim[o]; pa[r][4] = p.Cre[o]; pa[r][5] = p.Cim[o];
    }
    const float dt = bflo(pk2(__expf(bflo(p.logdt[d])), 0.f));
    f32x4 pm[3];
#pragma unroll
    for (int r = 0; r < 3; ++r) {
      const f32x2 cb = cmul(f32x2{bflo(pa[r][4]), bflo(pa[r][5])}, f32x2{dt * bflo(pa[r][2]), dt * bflo(pa[r][3])});
      pm[r] = f32x4{dt * bflo(pa[r][0]) * 1.4426950408889634f, dt * bflo(pa[r][1]) * 0.15915494309189535f, cb.x, cb.y};
    }
    pm0 = pm[0]; pm1 = pm[1]; pm2 = pm[2];
  }
  constexpr int TP = 272;
  const int r16 = lane & 15, g = lane >> 4;
  f32x4 acc[3];
#pragma unroll
  for (int t = 0; t < 3; ++t) acc[t] = f32x4{0.f, 0.f, 0.f, 0.f};
#pragma unroll 1
  for (int r = 0; r < 3; ++r) {
    const float ar2 = pm0.x, af = pm0.y;
    const f32x2 cb = {pm0.z, pm0.w};
    pm0 = pm1; pm1 = pm2;
    LAS unsigned *tb = (LAS unsigned *)kc + lane;
#define TBL(row) tb[(row) * (TP / 4)]
    {
      f32x2 e8 = cexp_l(ar2, af, 8.f); e8.y = -e8.y;
      f32x2 z = {cb.x, -cb.y};
      TBL(0) = pk2(z);
#pragma unroll
      for (int j = 1; j < 16; ++j) { z = cmul(z, e8); TBL(j) = pk2(z); }
    }
    {
      const f32x2 e1 = cexp_l(ar2, af, 1.f), e128 = cexp_l(ar2, af, 128.f);
      f32x2 ep[8];
      ep[1] = e1;
#pragma unroll
      for (int i = 2; i < 8; ++i) ep[i] = cmul(ep[i - 1], e1);
      TBL(16) = 0x00003f80u;
#pragma unroll
      for (int i = 1; i < 8; ++i) TBL(16 + i) = pk2(ep[i]);
      f32x2 eh = e128;
      TBL(24) = pk2(eh);
#pragma unroll
      for (int i = 1; i < 8; ++i) TBL(24 + i) = pk2(cmul(ep[i], e128));
#pragma unroll
      for (int h = 0; h < 2; ++h) {
        eh = cmul(eh, e128);
        TBL(32 + 4 * h) = pk2(eh);
#pragma unroll
        for (int i = 1; i < 4; ++i) TBL(32 + 4 * h + i) = pk2(cmul(ep[2 * i], eh));
      }
#pragma unroll
      for (int h = 0; h < 4; ++h) {
        eh = cmul(eh, e128);
        TBL(40 + 2 * h) = pk2(eh);
        TBL(41 + 2 * h) = pk2(cmul(eh, ep[4]));
      }
#pragma unroll
      for (int h = 0; h < 9; ++h) { eh = cmul(eh, e128); TBL(48 + h) = pk2(eh); }
    }
#undef TBL
#pragma unroll
    for (int s = 0; s < 4; ++s) {
      LAS unsigned char *src = kc + r16 * TP + s * 64 + g * 16;
      const v4u fb = *(LAS v4u *)src;
#pragma unroll
      for (int t = 0; t < 3; ++t) acc[t] = mfma(*(LAS v4u *)(src + (t + 1) * 16 * TP), fb, acc[t]);
    }
  }
  {
    *(LAS v2u *)(cmp + (128 * (g >> 1) + 4 * (g & 1) + 8 * r16) * 2) = v2u{pk2(acc[0].x, acc[0].y), pk2(acc[0].z, acc[0].w)};
    const int i1 = g < 2 ? 256 + 64 * g + 4 * r16 : 384 + 64 * (g - 2) + 2 * r16;
    *(LAS unsigned *)(cmp + i1 * 2) = pk2(acc[1].x, acc[1].y);
    *(LAS unsigned *)(cmp + i1 * 2 + (g < 2 ? 4 : 64)) = pk2(acc[1].z, acc[1].w);
    const unsigned a = pk2(acc[2].x, acc[2].y), b = pk2(acc[2].z, acc[2].w);
    LAS u16 *c2 = (LAS u16 *)cmp + 512 + 64 * g + r16;
    c2[0] = (u16)a; c2[16] = (u16)(a >> 16); c2[32] = (u16)b; c2[48] = (u16)(b >> 16);
    if (lane == 0) *(LAS unsigned *)(cmp - 4) = 0u;
  }
  {
    LAS u16 *c = (LAS u16 *)cmp;
    v4u wq[4], aq[4];
#pragma unroll
    for (int k = 0; k < 2; ++k) {
      const int q = 255 - lane - 64 * k;
      const unsigned H = c[q + 385], Lo = c[q + 384];
      const unsigned HH = H | (H << 16), LL = Lo | (Lo << 16), HL = H | (Lo << 16);
      const bool qe = (lane & 1) != 0;
      wq[k] = v4u{HH, qe ? HL : HH, LL, LL};
      aq[k] = v4u{HH, qe ? LL : HL, LL, LL};
    }
    {
      const int q = 127 - lane;
      const unsigned V0 = c[256 + 2 * q], V1 = c[257 + 2 * q], V2 = c[258 + 2 * q];
      wq[2] = v4u{V2 | (V2 << 16), V1 | (V1 << 16), V1 | (V0 << 16), V0 | (V0 << 16)};
      aq[2] = v4u{V2 | (V1 << 16), V1 | (V1 << 16), V0 | (V0 << 16), V0 | (V0 << 16)};
    }
    {
      const int q = 63 - lane, hb = lane >> 5;
      const int base = hb ? 8 * q : 128 + 4 * q;
      constexpr int offA[9] = {4, 3, 2, 2, 2, 1, 0, 0, 0}, offB[9] = {7, 6, 5, 4, 3, 2, 1, 0, -1};
      unsigned v[9];
#pragma unroll
      for (int e = 0; e < 8; ++e) v[e] = c[base + offA[e] + hb * (offB[e] - offA[e])];
      v[8] = c[base - (lane >= 31 ? 1 : 0)];
      wq[3] = v4u{v[0] | (v[1] << 16), v[2] | (v[3] << 16), v[4] | (v[5] << 16), v[6] | (v[7] << 16)};
      aq[3] = v4u{v[1] | (v[2] << 16), v[3] | (v[4] << 16), v[5] | (v[6] << 16), v[7] | (v[8] << 16)};
    }
    LAS unsigned char *o = kc + 16 + lane * 16;
#pragma unroll
    for (int k = 0; k < 4; ++k) {
      *(LAS v4u *)(o + k * 1024) = wq[k];
      *(LAS v4u *)(o + k * 1024 + CV_KP) = aq[k];
      LAS unsigned *o2 = (LAS unsigned *)(o + k * 1024 + 2 * CV_KP - 4), *o3 = (LAS unsigned *)(o + k * 1024 + 3 * CV_KP - 4);
      o2[0] = wq[k].x; o2[1] = wq[k].y; o2[2] = wq[k].z; o2[3] = wq[k].w;
      o3[0] = aq[k].x; o3[1] = aq[k].y; o3[2] = aq[k].z; o3[3] = aq[k].w;
    }
    if (lane < 32) {
      const int cpy = lane >> 3;
      *(LAS unsigned *)(kc + 16 + cpy * CV_KP + 4096 - (cpy >= 2 ? 4 : 0) + (lane & 7) * 4) = 0u;
    }
  }
}

__device__ __forceinline__ void phase_conv(const Params &p, LAS unsigned char *lds, int c4) {
  const int tid = threadIdx.x, lane = tid & 63, w = __builtin_amdgcn_readfirstlane(tid >> 6);
  const int r16 = lane & 15, g = lane >> 4;
  LAS unsigned char *kc = lds + CV_KC + w * CV_KCW;
  LAS unsigned char *fbase = kc + 16 + ((3 - r16) & 3) * CV_KP + (4056 - 3584) + 16 * g - 8 * (r16 >> 2);
  f32x4 acc[8];
  {
    const int th = 2 * r16 - 2 + g;
    LAS unsigned char *eb = lds + CV_UE + 4096 + th * 128 + ((4 + w + (th >> 1)) & 7) * 16;
    const unsigned one = (g == 2 + (r16 >> 3)) ? (0x3f80u << (16 * (r16 & 1))) : 0u;
    const int dw = (r16 & 7) >> 1;
    const v4u fid = {dw == 0 ? one : 0u, dw == 1 ? one : 0u, dw == 2 ? one : 0u, dw == 3 ? one : 0u};
#pragma unroll
    for (int J = 0; J < 8; ++J) acc[J] = mfma(fid, *(LAS v4u *)(eb + J * 4096), f32x4{0.f, 0.f, 0.f, 0.f});
  }
#pragma unroll 2
  for (int rho = 0; rho < 8; ++rho) {
    const int th = 2 * r16 - 4 * rho - 2 + g;
    LAS unsigned char *gb = lds + CV_UE + 4096 + th * 128 + ((w + (th >> 1)) & 7) * 16;
    LAS unsigned char *fb = fbase - 64 * rho;
    v4u Gf[8], Ff[8];
#pragma unroll
    for (int i = 0; i < 8; ++i) {
      Gf[i] = *(LAS v4u *)(gb + i * 4096);
      const v2u a = *(LAS v2u *)(fb + (7 - i) * 512), b = *(LAS v2u *)(fb + (7 - i) * 512 + 8);
      Ff[i] = v4u{a.x, a.y, b.x, b.y};
    }
#pragma unroll
    for (int Dd = 0; Dd < 8; ++Dd)
#pragma unroll
      for (int Jp = 0; Jp + Dd < 8; ++Jp) acc[Dd + Jp] = mfma(Ff[Dd], Gf[Jp], acc[Dd + Jp]);
  }
#pragma unroll
  for (int k = 0; k < 4; ++k)
    *(LAS v4u *)(kc + k * 1024 + lane * 16) = v4u{pk2(acc[2 * k].x, acc[2 * k].y), pk2(acc[2 * k].z, acc[2 * k].w),
                                                  pk2(acc[2 * k + 1].x, acc[2 * k + 1].y), pk2(acc[2 * k + 1].z, acc[2 * k + 1].w)};
  BARRIER();
  v4u v[4];
#pragma unroll
  for (int c = 0; c < 4; ++c) v[c] = *(LAS v4u *)(lds + CV_KC + c * CV_KCW + w * 1024 + lane * 16);
#pragma unroll
  for (int z = 0; z < 4; ++z) {
    const v4u ch = {__builtin_amdgcn_perm(v[1][z], v[0][z], 0x05040100u), __builtin_amdgcn_perm(v[3][z], v[2][z], 0x05040100u),
                    __builtin_amdgcn_perm(v[1][z], v[0][z], 0x07060302u), __builtin_amdgcn_perm(v[3][z], v[2][z], 0x07060302u)};
    const int rt = 4 * (2 * w + (z >> 1)) + (r16 >> 2);
    *(GAS v4u *)(p.ws + OFF_H + ((rt * 160 + c4) * 32 + (z & 1) * 16 + 4 * (r16 & 3) + g) * 16) = ch;
  }
}

constexpr int G_X = 100 * 1024;
constexpr int G_S = G_X + 20 * 1024;
constexpr int G12_BYTES = G_S + 4096;

__device__ __forceinline__ void store_frag5(unsigned char *base, int tq, int j, int g, const v2u (&pk)[5]) {
  const int ks0 = (5 * j + (j & 1)) >> 1;
  unsigned char *o = base + (((tq >> 6) * 20 + ks0) * 4 + (tq & 3)) * 1024 + (((tq >> 2) & 15) + 16 * g) * 16;
  *(GAS v4u *)(o) = v4u{pk[0].x, pk[0].y, pk[1].x, pk[1].y};
  *(GAS v4u *)(o + 4096) = v4u{pk[2].x, pk[2].y, pk[3].x, pk[3].y};
  *(GAS v2u *)(o + ((j & 1) ? -4096 + 8 : 8192)) = pk[4];
}
__device__ __forceinline__ int ntile5(int j, int i) { return (j & 1) ? (i < 4 ? 5 * j + 1 + i : 5 * j) : 5 * j + i; }

__device__ __forceinline__ void phase_g1(const Params &p, LAS unsigned char *lds, int rt, int j) {
  const int tid = threadIdx.x, lane = tid & 63, w = __builtin_amdgcn_readfirstlane(tid >> 6);
  const int r = lane & 15, g = lane >> 4, ps = w & 1, kh = w >> 1;
  dma_run<25>(p.ws + OFF_WT1 + (j * 100 + w * 25) * 1024 + lane * 16, lds_addr(lds) + w * 25 * 1024);
  const GAS unsigned char *ab = (const GAS unsigned char *)p.ws + OFF_H + ((rt * 160 + kh * 80 + g) * 32 + ps * 16 + r) * 16;
  v4u la[10], lb[10];
#pragma unroll
  for (int s = 0; s < 10; ++s) { la[s] = *(const GAS v4u *)(ab + s * 4096); lb[s] = *(const GAS v4u *)(ab + s * 4096 + 2048); }
  f32x4 cs[5];
#pragma unroll
  for (int i = 0; i < 5; ++i) cs[i] = *(const GAS f32x4 *)(p.ws + OFF_CS1 + (ntile5(j, i) * 16 + 4 * g) * 4);
  SCHED();
  WAIT_VM(25);
  BARRIER();
  f32x4 acc[2][5];
#pragma unroll
  for (int i = 0; i < 5; ++i) { acc[0][i] = f32x4{0.f, 0.f, 0.f, 0.f}; acc[1][i] = f32x4{0.f, 0.f, 0.f, 0.f}; }
  float s0 = 0.f, q0 = 0.f, s1 = 0.f, q1 = 0.f;
  LAS unsigned char *wb = lds + kh * 50 * 1024 + lane * 16;
#pragma unroll
  for (int s = 0; s < 10; ++s) {
    const v4u fa = {la[s].x, la[s].y, lb[s].x, lb[s].y}, fb = {la[s].z, la[s].w, lb[s].z, lb[s].w};
#pragma unroll
    for (int i = 0; i < 5; ++i) {
      const v4u wf = *(LAS v4u *)(wb + (s * 5 + i) * 1024);
      acc[0][i] = mfma(wf, fa, acc[0][i]);
      acc[1][i] = mfma(wf, fb, acc[1][i]);
    }
#pragma unroll
    for (int e = 0; e < 4; ++e) {
      s0 = dot2(fa[e], 0x3f803f80u, s0); q0 = dot2(fa[e], fa[e], q0);
      s1 = dot2(fb[e], 0x3f803f80u, s1); q1 = dot2(fb[e], fb[e], q1);
    }
  }
  *(LAS f32x2 *)(lds + G_S + (((2 * ps) * 16 + r) * 8 + kh * 4 + g) * 8) = f32x2{s0, q0};
  *(LAS f32x2 *)(lds + G_S + (((2 * ps + 1) * 16 + r) * 8 + kh * 4 + g) * 8) = f32x2{s1, q1};
  LAS unsigned char *xo = lds + G_X + w * 5120 + lane * 16, *xi = lds + G_X + (w ^ 2) * 5120 + lane * 16;
  if (kh) {
#pragma unroll
    for (int i = 0; i < 5; ++i) *(LAS f32x4 *)(xo + i * 1024) = acc[0][i];
  } else {
#pragma unroll
    for (int i = 0; i < 5; ++i) *(LAS f32x4 *)(xo + i * 1024) = acc[1][i];
  }
  BARRIER();
  f32x4 v[5];
  if (kh) {
#pragma unroll
    for (int i = 0; i < 5; ++i) v[i] = acc[1][i] + *(LAS f32x4 *)(xi + i * 1024);
  } else {
#pragma unroll
    for (int i = 0; i < 5; ++i) v[i] = acc[0][i] + *(LAS f32x4 *)(xi + i * 1024);
  }
  const int q = 2 * ps + kh;
  LAS f32x4 *sp = (LAS f32x4 *)(lds + G_S + (q * 16 + r) * 64);
  const f32x4 st = (sp[0] + sp[1]) + (sp[2] + sp[3]);
  const float mean = (st.x + st.z) * (1.f / D);
  const float var = fmaxf((st.y + st.w) * (1.f / D) - mean * mean, 0.f);
  const float r1 = __builtin_amdgcn_rsqf(var + EPS);
  const float r2 = __builtin_amdgcn_rsqf(var * r1 * r1 + EPS);
  const float rr = r1 * r2, nm = -mean * rr;
  const int t = rt * 64 + 4 * r + q;
  if (j == 0 && g == 0) *(GAS f32x2 *)(p.ws + OFF_ST1 + t * 8) = f32x2{mean, r1};
  const int grp = j >> 1, sh = (grp & 1) ? -(1 + (grp >> 1)) : 1 + (grp >> 1);
  const int td = t + sh;
  const bool oob = (unsigned)td >= (unsigned)L;
  v2u pk[5];
#pragma unroll
  for (int i = 0; i < 5; ++i) {
    const f32x4 pre = v[i] * rr + cs[i] * nm;
    const f32x2 a = gelu2(f32x2{pre.x, pre.y}), b = gelu2(f32x2{pre.z, pre.w});
    pk[i] = v2u{oob ? 0u : pk2(a), oob ? 0u : pk2(b)};
  }
  store_frag5(p.ws + OFF_H1, td & (L - 1), j, g, pk);
}

__device__ __forceinline__ void phase_g2(const Params &p, LAS unsigned char *lds, int rt, int j) {
  const int tid = threadIdx.x, lane = tid & 63, w = __builtin_amdgcn_readfirstlane(tid >> 6);
  const int r = lane & 15, g = lane >> 4, ps = w & 1, kh = w >> 1, q = 2 * ps + kh;
  dma_run<25>(p.ws + OFF_WT2 + (j * 100 + w * 25) * 1024 + lane * 16, lds_addr(lds) + w * 25 * 1024);
  const GAS unsigned char *ab = (const GAS unsigned char *)p.ws + OFF_H1 + ((rt * 20 + kh * 10) * 4) * 1024 + lane * 16;
  v4u fa[10], fb[10];
#pragma unroll
  for (int s = 0; s < 10; ++s) { fa[s] = *(const GAS v4u *)(ab + s * 4096 + q * 1024); fb[s] = *(const GAS v4u *)(ab + s * 4096 + (q ^ 1) * 1024); }
  const int t = rt * 64 + 4 * r + q;
  v2u hv[5];
#pragma unroll
  for (int i = 0; i < 5; ++i)
    hv[i] = *(const GAS v2u *)(p.ws + OFF_H + ((rt * 160 + ntile5(j, i) * 4 + g) * 32 + (q >> 1) * 16 + r) * 16 + (q & 1) * 8);
  const f32x2 st = *(const GAS f32x2 *)(p.ws + OFF_ST1 + t * 8);
  SCHED();
  WAIT_VM(26);
  BARRIER();
  f32x4 acc[2][5];
#pragma unroll
  for (int i = 0; i < 5; ++i) { acc[0][i] = f32x4{0.f, 0.f, 0.f, 0.f}; acc[1][i] = f32x4{0.f, 0.f, 0.f, 0.f}; }
  LAS unsigned char *wb = lds + kh * 50 * 1024 + lane * 16;
#pragma unroll
  for (int s = 0; s < 10; ++s) {
#pragma unroll
    for (int i = 0; i < 5; ++i) {
      const v4u wf = *(LAS v4u *)(wb + (s * 5 + i) * 1024);
      acc[0][i] = mfma(wf, fa[s], acc[0][i]);
      acc[1][i] = mfma(wf, fb[s], acc[1][i]);
    }
  }
  LAS unsigned char *xo = lds + G_X + w * 5120 + lane * 16, *xi = lds + G_X + (w ^ 2) * 5120 + lane * 16;
#pragma unroll
  for (int i = 0; i < 5; ++i) *(LAS f32x4 *)(xo + i * 1024) = acc[1][i];
  BARRIER();
  v2u pk[5];
  const float nm = -st.x * st.y;
#pragma unroll
  for (int i = 0; i < 5; ++i) {
    const f32x4 hs = acc[0][i] + *(LAS f32x4 *)(xi + i * 1024);
    const f32x4 h4 = {bflo(hv[i].x), bfhi(hv[i].x), bflo(hv[i].y), bfhi(hv[i].y)};
    const f32x4 x1 = (h4 * st.y + nm) + hs;
    pk[i] = v2u{pk2(x1.x, x1.y), pk2(x1.z, x1.w)};
  }
  store_frag5(p.ws + OFF_X1, t, j, g, pk);
}

__device__ __forceinline__ void phase_g3(const Params &p, LAS unsigned char *lds, int rt2, int j3) {
  const int tid = threadIdx.x, lane = tid & 63, w = __builtin_amdgcn_readfirstlane(tid >> 6);
  const int r = lane & 15, g = lane >> 4, rt = 2 * rt2 + (w >> 1), q0 = 2 * (w & 1);
  dma_run<30>(p.ws + OFF_WT3 + (j3 * 120 + w * 30) * 1024 + lane * 16, lds_addr(lds) + w * 30 * 1024);
  const GAS unsigned char *ab = (const GAS unsigned char *)p.ws + OFF_X1 + (rt * 80 + q0) * 1024 + lane * 16;
  constexpr int WIN = 12;
  v4u fa[WIN], fb[WIN];
#pragma unroll
  for (int s = 0; s < WIN; ++s) { fa[s] = *(const GAS v4u *)(ab + s * 4096); fb[s] = *(const GAS v4u *)(ab + s * 4096 + 1024); }
  SCHED();
  WAIT_VM(24);
  BARRIER();
  f32x4 acc[2][6];
#pragma unroll
  for (int i = 0; i < 6; ++i) { acc[0][i] = f32x4{0.f, 0.f, 0.f, 0.f}; acc[1][i] = f32x4{0.f, 0.f, 0.f, 0.f}; }
  float s0 = 0.f, qq0 = 0.f, s1 = 0.f, qq1 = 0.f;
  LAS unsigned char *wb = lds + lane * 16;
#pragma unroll
  for (int s = 0; s < 20; ++s) {
    const v4u a = fa[s % WIN], b = fb[s % WIN];
    if (s + WIN < 20) { fa[s % WIN] = *(const GAS v4u *)(ab + (s + WIN) * 4096); fb[s % WIN] = *(const GAS v4u *)(ab + (s + WIN) * 4096 + 1024); }
#pragma unroll
    for (int i = 0; i < 6; ++i) {
      const v4u wf = *(LAS v4u *)(wb + (s * 6 + i) * 1024);
      acc[0][i] = mfma(wf, a, acc[0][i]);
      acc[1][i] = mfma(wf, b, acc[1][i]);
    }
#pragma unroll
    for (int e = 0; e < 4; ++e) {
      s0 = dot2(a[e], 0x3f803f80u, s0); qq0 = dot2(a[e], a[e], qq0);
      s1 = dot2(b[e], 0x3f803f80u, s1); qq1 = dot2(b[e], b[e], qq1);
    }
    if (s + WIN < 20) SCHED();
  }
  f32x4 cs[6];
#pragma unroll
  for (int i = 0; i < 6; ++i) cs[i] = *(const GAS f32x4 *)(p.ws + OFF_CS3 + ((j3 * 6 + i) * 16 + 4 * g) * 4);
  s0 = sum_x32(sum_x16(s0)); qq0 = sum_x32(sum_x16(qq0)); s1 = sum_x32(sum_x16(s1)); qq1 = sum_x32(sum_x16(qq1));
#pragma unroll
  for (int m = 0; m < 2; ++m) {
    const float mean = (m ? s1 : s0) * (1.f / D);
    const float var = fmaxf((m ? qq1 : qq0) * (1.f / D) - mean * mean, 0.f);
    const float rr = __builtin_amdgcn_rsqf(var + EPS), nm = -mean * rr;
    unsigned char *o = p.ws + OFF_HID + ((rt * 48 + j3 * 3) * 4 + q0 + m) * 1024 + lane * 16;
#pragma unroll
    for (int k = 0; k < 3; ++k) {
      const f32x4 pa = acc[m][2 * k] * rr + cs[2 * k] * nm, pb = acc[m][2 * k + 1] * rr + cs[2 * k + 1] * nm;
      *(GAS v4u *)(o + k * 4096) = v4u{pk2(gelu2(f32x2{pa.x, pa.y})), pk2(gelu2(f32x2{pa.z, pa.w})), pk2(gelu2(f32x2{pb.x, pb.y})),
                                       pk2(gelu2(f32x2{pb.z, pb.w}))};
    }
  }
}

__device__ __forceinline__ void phase_g4(const Params &p, LAS unsigned char *lds, int rt, int j) {
  const int tid = threadIdx.x, lane = tid & 63, w = __builtin_amdgcn_readfirstlane(tid >> 6);
  const int r = lane & 15, g = lane >> 4;
  const GAS unsigned char *ab = (const GAS unsigned char *)p.ws + OFF_HID + ((rt * 48 + w * 12) * 4) * 1024 + lane * 16;
  const GAS unsigned char *wb = (const GAS unsigned char *)p.ws + OFF_WT4 + ((j * 48 + w * 12) * 5) * 1024 + lane * 16;
  constexpr int WIN = 5;
  v4u fa[WIN][4], fw[WIN][5];
#pragma unroll
  for (int s = 0; s < WIN; ++s) {
#pragma unroll
    for (int i = 0; i < 5; ++i) fw[s][i] = *(const GAS v4u *)(wb + (s * 5 + i) * 1024);
#pragma unroll
    for (int m = 0; m < 4; ++m) fa[s][m] = *(const GAS v4u *)(ab + s * 4096 + ((w + m) & 3) * 1024);
  }
  SCHED();
  f32x4 acc[4][5];
#pragma unroll
  for (int m = 0; m < 4; ++m)
#pragma unroll
    for (int i = 0; i < 5; ++i) acc[m][i] = f32x4{0.f, 0.f, 0.f, 0.f};
#pragma unroll
  for (int s = 0; s < 12; ++s) {
#pragma unroll
    for (int m = 0; m < 4; ++m)
#pragma unroll
      for (int i = 0; i < 5; ++i) acc[m][i] = mfma(fw[s % WIN][i], fa[s % WIN][m], acc[m][i]);
    if (s + WIN < 12) {
#pragma unroll
      for (int i = 0; i < 5; ++i) fw[s % WIN][i] = *(const GAS v4u *)(wb + ((s + WIN) * 5 + i) * 1024);
#pragma unroll
      for (int m = 0; m < 4; ++m) fa[s % WIN][m] = *(const GAS v4u *)(ab + (s + WIN) * 4096 + ((w + m) & 3) * 1024);
    }
    SCHED();
  }
  v2u hv[5], xv[5];
#pragma unroll
  for (int i = 0; i < 5; ++i) {
    const int nt = ntile5(j, i);
    hv[i] = *(const GAS v2u *)(p.ws + OFF_H + ((rt * 160 + nt * 4 + g) * 32 + (w >> 1) * 16 + r) * 16 + (w & 1) * 8);
    xv[i] = *(const GAS v2u *)(p.ws + OFF_X1 + ((rt * 20 + (nt >> 1)) * 4 + w) * 1024 + lane * 16 + (nt & 1) * 8);
  }
#pragma unroll
  for (int m = 1; m < 4; ++m)
#pragma unroll
    for (int i = 0; i < 5; ++i) *(LAS f32x4 *)(lds + ((((w + m) & 3) * 3 + m - 1) * 5 + i) * 1024 + lane * 16) = acc[m][i];
  BARRIER();
  u16 *orow = p.out + (size_t)(rt * 64 + 4 * r + w) * D + 4 * g;
#pragma unroll
  for (int i = 0; i < 5; ++i) {
    LAS unsigned char *xi = lds + (w * 15 + i) * 1024 + lane * 16;
    const f32x4 h2 = (acc[0][i] + *(LAS f32x4 *)(xi)) + (*(LAS f32x4 *)(xi + 5120) + *(LAS f32x4 *)(xi + 10240));
    const f32x4 h4 = {bflo(hv[i].x), bfhi(hv[i].x), bflo(hv[i].y), bfhi(hv[i].y)};
    const f32x4 x4 = {bflo(xv[i].x), bfhi(xv[i].x), bflo(xv[i].y), bfhi(xv[i].y)};
    const f32x4 o = h4 + (x4 + h2);
    *(GAS v2u *)(orow + ntile5(j, i) * 16) = v2u{pk2(o.x, o.y), pk2(o.z, o.w)};
  }
}

__global__ void __launch_bounds__(256) k_front(Params p) {
  __shared__ __attribute__((aligned(16))) unsigned char smem[81920];
  const int b = blockIdx.x;
  if (b < 74) phase_retile(p, (LAS unsigned char *)smem, b);
  else phase_ln(p, (LAS unsigned char *)smem, b - 74);
}
__global__ void __launch_bounds__(256) k_conv(Params p) {
  __shared__ __attribute__((aligned(16))) unsigned char smem[CV_BYTES];
  LAS unsigned char *lds = (LAS unsigned char *)smem;
  const int c4 = blockIdx.x, tid = threadIdx.x, w = __builtin_amdgcn_readfirstlane(tid >> 6);
  dma_run<8>(p.ws + OFF_UE + c4 * 32768 + w * 8192 + (tid & 63) * 16, lds_addr(lds) + CV_UE + 4096 + w * 8192);
  *(LAS v4u *)(lds + CV_UE + tid * 16) = v4u{0u, 0u, 0u, 0u};
  phase_kgen(p, lds, c4);
  WAIT_VM(0);
  BARRIER();
  phase_conv(p, lds, c4);
}
__global__ void __launch_bounds__(256) k_g1(Params p) {
  __shared__ __attribute__((aligned(16))) unsigned char smem[G12_BYTES];
  const int b = blockIdx.x, idx = b >> 3;
  phase_g1(p, (LAS unsigned char *)smem, 4 * (b & 7) + (idx >> 3), idx & 7);
}
__global__ void __launch_bounds__(256) k_g2(Params p) {
  __shared__ __attribute__((aligned(16))) unsigned char smem[G12_BYTES];
  const int b = blockIdx.x, idx = b >> 3;
  phase_g2(p, (LAS unsigned char *)smem, 4 * (b & 7) + (idx >> 3), idx & 7);
}
__global__ void __launch_bounds__(256) k_g3(Params p) {
  __shared__ __attribute__((aligned(16))) unsigned char smem[120 * 1024];
  const int b = blockIdx.x, idx = b >> 3;
  phase_g3(p, (LAS unsigned char *)smem, 2 * (b & 7) + (idx >> 4), idx & 15);
}
__global__ void __launch_bounds__(256) k_g4(Params p) {
  __shared__ __attribute__((aligned(16))) unsigned char smem[60 * 1024];
  const int b = blockIdx.x, idx = b >> 3;
  phase_g4(p, (LAS unsigned char *)smem, 4 * (b & 7) + (idx >> 3), idx & 7);
}

extern "C" void kernel_launch(void *const *d_in, const int *in_sizes, int n_in, void *d_out, int out_size, void *d_ws,
                              size_t ws_size, hipStream_t stream) {
  Params p;
  p.x = (const u16 *)d_in[0]; p.Are = (const u16 *)d_in[1]; p.Aim = (const u16 *)d_in[2]; p.Bre = (const u16 *)d_in[3];
  p.Bim = (const u16 *)d_in[4]; p.Cre = (const u16 *)d_in[5]; p.Cim = (const u16 *)d_in[6]; p.logdt = (const u16 *)d_in[7];
  p.Dsk = (const u16 *)d_in[8]; p.w1 = (const u16 *)d_in[10]; p.w2 = (const u16 *)d_in[11]; p.w3 = (const u16 *)d_in[12];
  p.w4 = (const u16 *)d_in[13]; p.out = (u16 *)d_out; p.ws = (unsigned char *)d_ws;
  k_front<<<74 + 256, 256, 0, stream>>>(p);
  k_conv<<<160, 256, 0, stream>>>(p);
  k_g1<<<256, 256, 0, stream>>>(p);
  k_g2<<<256, 256, 0, stream>>>(p);
  k_g3<<<256, 256, 0, stream>>>(p);
  k_g4<<<256, 256, 0, stream>>>(p);
}
```

```cpp
#include <hip/hip_runtime.h>
#include <hip/hip_bf16.h>
#include <stdint.h>

#define GAS __attribute__((address_space(1)))
#define LAS __attribute__((address_space(3)))
typedef unsigned short u16;
typedef unsigned long long u64;
typedef unsigned v4u __attribute__((ext_vector_type(4)));
typedef v4u v4u_a8 __attribute__((aligned(8)));
typedef unsigned v2u __attribute__((ext_vector_type(2)));
typedef float f32x4 __attribute__((ext_vector_type(4)));
typedef float f32x2 __attribute__((ext_vector_type(2)));
typedef short bf16x8 __attribute__((ext_vector_type(8)));
typedef short s16x4 __attribute__((ext_vector_type(4)));
typedef __bf16 bf16x2 __attribute__((ext_vector_type(2)));
typedef __amdgpu_buffer_rsrc_t rsrc_t;

constexpr int L = 2048, D = 640, NM = 192, F = 1536;
constexpr float EPS = 1e-5f;

constexpr int F_LN = 1024, F_W = F_LN + 8192, F_CV = F_W + 3072, F_G1 = F_CV + 5120, F_G2 = F_G1 + 8192, F_G3 = F_G2 + 8192;
constexpr int OFF_WT1 = 65536;
constexpr int OFF_WT2 = OFF_WT1 + 800 * 1024;
constexpr int OFF_WT3 = OFF_WT2 + 800 * 1024;
constexpr int OFF_WT4 = OFF_WT3 + 1920 * 1024;
constexpr int OFF_UE = OFF_WT4 + 1920 * 1024 + 4096 + 8192 + 16384;
constexpr int OFF_H = OFF_UE + 160 * 256 * 128;
constexpr int OFF_H1 = OFF_H + 32 * 160 * 512;
constexpr int OFF_X1 = OFF_H1 + 32 * 20 * 4096;
constexpr int OFF_HID = OFF_X1 + 32 * 20 * 4096;
constexpr int WS_BYTES = OFF_HID + 32 * 48 * 4096;

constexpr int CV_UE = 0, CV_KC = 36864, CV_KCW = 16640, CV_KP = 4128, CV_CMP = CV_KC + 4 * CV_KCW, CV_CMPW = 1568;
constexpr int LN_ST = CV_CMP + 4 * CV_CMPW, LN_PITCH = 1280 + 16, LN_CNT = 131072;
constexpr int CV_X = 112 * 1024;
constexpr int G_X = 120 * 1024, G_S = 140 * 1024, G_CS = 144 * 1024, G4_X = 140 * 1024;
constexpr int LDS_BYTES = 160 * 1024;

struct Params {
  const u16 *x, *Are, *Aim, *Bre, *Bim, *Cre, *Cim, *logdt, *Dsk, *w1, *w2, *w3, *w4;
  u16 *out;
  unsigned char *ws;
};

__device__ __forceinline__ float bflo(unsigned v) { return __builtin_bit_cast(float, v << 16); }
__device__ __forceinline__ float bfhi(unsigned v) { return __builtin_bit_cast(float, v & 0xffff0000u); }
__device__ __forceinline__ unsigned pk2(float a, float b) {
  return __builtin_bit_cast(unsigned, __builtin_convertvector(f32x2{a, b}, bf16x2));
}
__device__ __forceinline__ unsigned pk2(f32x2 a) { return __builtin_bit_cast(unsigned, __builtin_convertvector(a, bf16x2)); }
__device__ __forceinline__ f32x4 mfma(v4u a, v4u b, f32x4 c) {
  return __builtin_amdgcn_mfma_f32_16x16x32_bf16(__builtin_bit_cast(bf16x8, a), __builtin_bit_cast(bf16x8, b), c, 0, 0, 0);
}
__device__ __forceinline__ float dot2(unsigned a, unsigned b, float c) {
  return __builtin_amdgcn_fdot2_f32_bf16(__builtin_bit_cast(bf16x2, a), __builtin_bit_cast(bf16x2, b), c, false);
}
__device__ __forceinline__ f32x2 cmul(f32x2 x, f32x2 y) {
  f32x2 t = f32x2{x.x, x.x} * y;
  return __builtin_elementwise_fma(f32x2{x.y, x.y}, f32x2{-y.y, y.x}, t);
}
template <int CTRL> __device__ __forceinline__ float dpp_add(float x) {
  return x + __builtin_bit_cast(float, __builtin_amdgcn_update_dpp(0, __builtin_bit_cast(int, x), CTRL, 0xf, 0xf, false));
}
__device__ __forceinline__ float sum16(float x) {
  x = dpp_add<0x128>(x); x = dpp_add<0x124>(x); x = dpp_add<0x122>(x); x = dpp_add<0x121>(x);
  return x;
}
__device__ __forceinline__ float sum_x16(float x) {
  const unsigned a = __builtin_bit_cast(unsigned, x);
  auto s = __builtin_amdgcn_permlane16_swap(a, a, false, false);
  const unsigned s0 = s[0], s1 = s[1];
  return __builtin_bit_cast(float, s0) + __builtin_bit_cast(float, s1);
}
__device__ __forceinline__ float sum_x32(float x) {
  const unsigned a = __builtin_bit_cast(unsigned, x);
  auto s = __builtin_amdgcn_permlane32_swap(a, a, false, false);
  const unsigned s0 = s[0], s1 = s[1];
  return __builtin_bit_cast(float, s0) + __builtin_bit_cast(float, s1);
}
__device__ __forceinline__ v2u tr_read(LAS unsigned char *p) {
  return __builtin_bit_cast(v2u, __builtin_amdgcn_ds_read_tr16_b64_v4i16((LAS s16x4 *)p));
}
__device__ __forceinline__ void dma1(const unsigned char *gsrc, unsigned ldst) {
  unsigned keep;
  asm volatile("s_mov_b32 %0, m0\n\ts_mov_b32 m0, %2\n\ts_nop 0\n\tglobal_load_lds_dwordx4 %1, off\n\ts_mov_b32 m0, %0"
               : "=&s"(keep) : "v"(gsrc), "s"(ldst) : "memory");
}
#define DRAIN() asm volatile("s_waitcnt vmcnt(0)" ::: "memory")
#define SCHED() __builtin_amdgcn_sched_barrier(0)
#define BARRIER() do { SCHED(); asm volatile("s_waitcnt lgkmcnt(0)\n\ts_barrier" ::: "memory"); SCHED(); } while (0)
#define SGB(mask, n) __builtin_amdgcn_sched_group_barrier(mask, n, 0)
constexpr int SC1 = 16;
__device__ __forceinline__ v4u ld16(rsrc_t rs, int voff, int soff) { return __builtin_amdgcn_raw_buffer_load_b128(rs, voff, soff, SC1); }
__device__ __forceinline__ v2u ld8(rsrc_t rs, int voff, int soff) { return __builtin_amdgcn_raw_buffer_load_b64(rs, voff, soff, SC1); }
__device__ __forceinline__ void st16(rsrc_t rs, int voff, int soff, v4u v) { __builtin_amdgcn_raw_buffer_store_b128(v, rs, voff + soff, 0, SC1); }
__device__ __forceinline__ void st8(rsrc_t rs, int voff, int soff, v2u v) { __builtin_amdgcn_raw_buffer_store_b64(v, rs, voff, soff, SC1); }
__device__ __forceinline__ unsigned lds_addr(LAS unsigned char *p) { return (unsigned)(size_t)p; }


__device__ __forceinline__ void publish(rsrc_t rs, int off, v2u tag, int lane) {
  DRAIN();
  if (lane == 0) st8(rs, off, 0, tag);
}
constexpr unsigned SPIN_CAP = 1u << 17;
template <int SLEEP> __device__ __forceinline__ void wait8(rsrc_t rs, int off, v2u tag) {
#pragma unroll 1
  for (unsigned spins = 0; spins < SPIN_CAP; ++spins) {
    const v2u v = ld8(rs, off, 0);
    if (__all(v.x == tag.x && v.y == tag.y)) break;
    __builtin_amdgcn_s_sleep(SLEEP);
  }
  asm volatile("" ::: "memory");
}
template <int SLEEP> __device__ __forceinline__ void wait16(rsrc_t rs, int off, v2u tag) {
#pragma unroll 1
  for (unsigned spins = 0; spins < SPIN_CAP; ++spins) {
    const v4u v = ld16(rs, off, 0);
    if (__all(v.x == tag.x && v.y == tag.y && v.z == tag.x && v.w == tag.y)) break;
    __builtin_amdgcn_s_sleep(SLEEP);
  }
  asm volatile("" ::: "memory");
}

__device__ __forceinline__ f32x2 gelu2(f32x2 x) {
  constexpr float c0 = -2.0f * 0.7978845608028654f * 1.4426950408889634f, c1 = c0 * 0.044715f;
  f32x2 t = __builtin_elementwise_fma(x * x, f32x2{c1, c1}, f32x2{c0, c0}) * x;
  f32x2 e = f32x2{__builtin_amdgcn_exp2f(t.x), __builtin_amdgcn_exp2f(t.y)} + f32x2{1.f, 1.f};
  return x * f32x2{__builtin_amdgcn_rcpf(e.x), __builtin_amdgcn_rcpf(e.y)};
}
constexpr unsigned ONE2 = 0x3f803f80u;
__device__ __forceinline__ int ntile5(int j, int i) { return (j & 1) ? (i < 4 ? 5 * j + 1 + i : 5 * j) : 5 * j + i; }
__device__ __forceinline__ int shift_of(int j) { const int grp = j >> 1; return (grp & 1) ? -(1 + (grp >> 1)) : 1 + (grp >> 1); }

__device__ __forceinline__ f32x2 cexp_l(float ar2, float af, float l) {
  const float m = __builtin_amdgcn_exp2f(ar2 * l), ph = __builtin_amdgcn_fractf(af * l);
  return f32x2{m * __builtin_amdgcn_cosf(ph), m * __builtin_amdgcn_sinf(ph)};
}

__device__ __forceinline__ void store_frag5(rsrc_t rs, int base, int tq, int j, int g, const v2u (&pk)[5]) {
  const int ks0 = (5 * j + (j & 1)) >> 1;
  const int o = ((tq >> 6) * 80 + (tq & 3)) * 1024 + (((tq >> 2) & 15) + 16 * g) * 16;
  const int so = base + ks0 * 4096;
  st16(rs, o, so, v4u{pk[0].x, pk[0].y, pk[1].x, pk[1].y});
  st16(rs, o, so + 4096, v4u{pk[2].x, pk[2].y, pk[3].x, pk[3].y});
  st8(rs, o, so + ((j & 1) ? -4096 + 8 : 8192), pk[4]);
}

__device__ __forceinline__ v2u get_tag(LAS unsigned char *lds) {
#pragma unroll 1
  for (unsigned spins = 0; spins < SPIN_CAP; ++spins)
    if (__hip_atomic_load((LAS unsigned *)(lds + LN_CNT), __ATOMIC_RELAXED, __HIP_MEMORY_SCOPE_WORKGROUP) >= 4u) break;
  asm volatile("" ::: "memory");
  const v2u t = *(LAS v2u *)(lds + LN_CNT + 8);
  return v2u{(unsigned)__builtin_amdgcn_readfirstlane(t.x), (unsigned)__builtin_amdgcn_readfirstlane(t.y)};
}
__device__ __forceinline__ void role_compute(const Params &p, LAS unsigned char *lds, rsrc_t rs, int w, int lane) {
  v2u tag;
  const int bid = blockIdx.x, xg = bid & 7, idx = bid >> 3;
  const bool conv = idx < 20;
  const int r16 = lane & 15, g = lane >> 4;
  if (conv) {
    const int c4 = xg * 20 + idx, d = c4 * 4 + w;
    LAS unsigned char *kc = lds + CV_KC + w * CV_KCW;
    LAS unsigned char *cmp = lds + CV_CMP + w * CV_CMPW + 16;
    f32x4 pm0, pm1, pm2;
    {
      unsigned pa[3][6];
#pragma unroll
      for (int r = 0; r < 3; ++r) {
        const int o = d * NM + r * 64 + lane;
        pa[r][0] = p.Are[o]; pa[r][1] = p.Aim[o]; pa[r][2] = p.Bre[o]; pa[r][3] = p.Bim[o]; pa[r][4] = p.Cre[o]; pa[r][5] = p.Cim[o];
      }
      const unsigned ldt = p.logdt[d];
      *(LAS v4u *)(lds + CV_UE + (w * 64 + lane) * 16) = v4u{0u, 0u, 0u, 0u};
      SCHED();
      BARRIER();
      const float dt = bflo(pk2(__expf(bflo(ldt)), 0.f));
      f32x4 pm[3];
#pragma unroll
      for (int r = 0; r < 3; ++r) {
        const f32x2 cb = cmul(f32x2{bflo(pa[r][4]), bflo(pa[r][5])}, f32x2{dt * bflo(pa[r][2]), dt * bflo(pa[r][3])});
        pm[r] = f32x4{dt * bflo(pa[r][0]) * 1.4426950408889634f, dt * bflo(pa[r][1]) * 0.15915494309189535f, cb.x, cb.y};
      }
      pm0 = pm[0]; pm1 = pm[1]; pm2 = pm[2];
    }
    constexpr int TP = 272;
    f32x4 kacc[3];
#pragma unroll
    for (int t = 0; t < 3; ++t) kacc[t] = f32x4{0.f, 0.f, 0.f, 0.f};
    v4u tf[4][4];
#pragma unroll
    for (int s = 0; s < 4; ++s)
#pragma unroll
      for (int t = 0; t < 4; ++t) tf[s][t] = v4u{0u, 0u, 0u, 0u};
#pragma unroll 1
    for (int r = 0; r < 3; ++r) {
      const float ar2 = pm0.x, af = pm0.y;
      const f32x2 cb = {pm0.z, pm0.w};
      pm0 = pm1; pm1 = pm2;
      LAS unsigned *tb = (LAS unsigned *)kc + lane;
#define TBL(row) tb[(row) * (TP / 4)]
      f32x2 e8 = cexp_l(ar2, af, 8.f); e8.y = -e8.y;
      const f32x2 e1 = cexp_l(ar2, af, 1.f), e128 = cexp_l(ar2, af, 128.f);
      SCHED();
#pragma unroll
      for (int s = 0; s < 4; ++s)
#pragma unroll
        for (int t = 0; t < 3; ++t) kacc[t] = mfma(tf[s][t + 1], tf[s][0], kacc[t]);
      {
        f32x2 z = {cb.x, -cb.y};
        TBL(0) = pk2(z);
#pragma unroll
        for (int j = 1; j < 16; ++j) { z = cmul(z, e8); TBL(j) = pk2(z); }
      }
      {
        f32x2 ep[8];
        ep[1] = e1;
#pragma unroll
        for (int i = 2; i < 8; ++i) ep[i] = cmul(ep[i - 1], e1);
        TBL(16) = 0x00003f80u;
#pragma unroll
        for (int i = 1; i < 8; ++i) TBL(16 + i) = pk2(ep[i]);
        f32x2 eh = e128;
        TBL(24) = pk2(eh);
#pragma unroll
        for (int i = 1; i < 8; ++i) TBL(24 + i) = pk2(cmul(ep[i], e128));
#pragma unroll
        for (int h = 0; h < 2; ++h) {
          eh = cmul(eh, e128);
          TBL(32 + 4 * h) = pk2(eh);
#pragma unroll
          for (int i = 1; i < 4; ++i) TBL(32 + 4 * h + i) = pk2(cmul(ep[2 * i], eh));
        }
#pragma unroll
        for (int h = 0; h < 4; ++h) {
          eh = cmul(eh, e128);
          TBL(40 + 2 * h) = pk2(eh);
          TBL(41 + 2 * h) = pk2(cmul(eh, ep[4]));
        }
#pragma unroll
        for (int h = 0; h < 9; ++h) { eh = cmul(eh, e128); TBL(48 + h) = pk2(eh); }
      }
#undef TBL
#pragma unroll
      for (int k = 0; k < 12; ++k) { SGB(0x2, 4); SGB(0x8, 1); }
      SCHED();
#pragma unroll
      for (int s = 0; s < 4; ++s)
#pragma unroll
        for (int t = 0; t < 4; ++t) tf[s][t] = *(LAS v4u *)(kc + (r16 + t * 16) * TP + s * 64 + g * 16);
      SCHED();
    }
#pragma unroll
    for (int s = 0; s < 4; ++s)
#pragma unroll
      for (int t = 0; t < 3; ++t) kacc[t] = mfma(tf[s][t + 1], tf[s][0], kacc[t]);
    {
      *(LAS v2u *)(cmp + (128 * (g >> 1) + 4 * (g & 1) + 8 * r16) * 2) = v2u{pk2(kacc[0].x, kacc[0].y), pk2(kacc[0].z, kacc[0].w)};
      const int i1 = g < 2 ? 256 + 64 * g + 4 * r16 : 384 + 64 * (g - 2) + 2 * r16;
      *(LAS unsigned *)(cmp + i1 * 2) = pk2(kacc[1].x, kacc[1].y);
      *(LAS unsigned *)(cmp + i1 * 2 + (g < 2 ? 4 : 64)) = pk2(kacc[1].z, kacc[1].w);
      const unsigned a = pk2(kacc[2].x, kacc[2].y), b = pk2(kacc[2].z, kacc[2].w);
      LAS u16 *c2 = (LAS u16 *)cmp + 512 + 64 * g + r16;
      c2[0] = (u16)a; c2[16] = (u16)(a >> 16); c2[32] = (u16)b; c2[48] = (u16)(b >> 16);
      if (lane == 0) *(LAS unsigned *)(cmp - 4) = 0u;
    }
    {
      LAS u16 *c = (LAS u16 *)cmp;
      v4u wq[4], aq[4];
#pragma unroll
      for (int k = 0; k < 2; ++k) {
        const int q = 255 - lane - 64 * k;
        const unsigned H = c[q + 385], Lo = c[q + 384];
        const unsigned HH = H | (H << 16), LL = Lo | (Lo << 16), HL = H | (Lo << 16);
        const bool qe = (lane & 1) != 0;
        wq[k] = v4u{HH, qe ? HL : HH, LL, LL};
        aq[k] = v4u{HH, qe ? LL : HL, LL, LL};
      }
      {
        const int q = 127 - lane;
        const unsigned V0 = c[256 + 2 * q], V1 = c[257 + 2 * q], V2 = c[258 + 2 * q];
        wq[2] = v4u{V2 | (V2 << 16), V1 | (V1 << 16), V1 | (V0 << 16), V0 | (V0 << 16)};
        aq[2] = v4u{V2 | (V1 << 16), V1 | (V1 << 16), V0 | (V0 << 16), V0 | (V0 << 16)};
      }
      {
        const int q = 63 - lane, hb = lane >> 5;
        const int base = hb ? 8 * q : 128 + 4 * q;
        constexpr int offA[9] = {4, 3, 2, 2, 2, 1, 0, 0, 0}, offB[9] = {7, 6, 5, 4, 3, 2, 1, 0, -1};
        unsigned v[9];
#pragma unroll
        for (int e = 0; e < 8; ++e) v[e] = c[base + offA[e] + hb * (offB[e] - offA[e])];
        v[8] = c[base - (lane >= 31 ? 1 : 0)];
        wq[3] = v4u{v[0] | (v[1] << 16), v[2] | (v[3] << 16), v[4] | (v[5] << 16), v[6] | (v[7] << 16)};
        aq[3] = v4u{v[1] | (v[2] << 16), v[3] | (v[4] << 16), v[5] | (v[6] << 16), v[7] | (v[8] << 16)};
      }
      if (lane < 28) {
        const int cpy = lane / 7;
        *(LAS unsigned *)(kc + 16 + cpy * CV_KP + 4096 - (cpy >= 2 ? 4 : 0) + (lane - cpy * 7) * 4) = 0u;
      }
      LAS unsigned char *o = kc + 16 + lane * 16;
#pragma unroll
      for (int k = 0; k < 4; ++k) {
        *(LAS v4u *)(o + k * 1024) = wq[k];
        *(LAS v4u *)(o + k * 1024 + CV_KP) = aq[k];
        LAS unsigned *o2 = (LAS unsigned *)(o + k * 1024 + 2 * CV_KP - 4), *o3 = (LAS unsigned *)(o + k * 1024 + 3 * CV_KP - 4);
        o2[0] = wq[k].x; o2[1] = wq[k].y; o2[2] = wq[k].z; o2[3] = wq[k].w;
        o3[0] = aq[k].x; o3[1] = aq[k].y; o3[2] = aq[k].z; o3[3] = aq[k].w;
      }
    }
    LAS unsigned char *gb[4];
#pragma unroll
    for (int k = 0; k < 4; ++k) {
      const int th = 2 * r16 - 4 * (k + 4) - 2 + g;
      gb[k] = lds + CV_UE + 4096 + th * 128 + ((w + (th >> 1)) & 7) * 16;
    }
    LAS unsigned char *eb;
    {
      const int th = 2 * r16 - 2 + g;
      eb = lds + CV_UE + 4096 + th * 128 + ((4 + w + (th >> 1)) & 7) * 16;
    }
    LAS unsigned char *f0 = kc + 16 + ((3 - r16) & 3) * CV_KP + (4056 - 3584 - 448) + 16 * g - 8 * (r16 >> 2), *f1 = f0 + 2048;
    asm("" : "+v"(f0));
    asm("" : "+v"(f1));
    const unsigned one = (g == 2 + (r16 >> 3)) ? (0x3f80u << (16 * (r16 & 1))) : 0u;
    const int dw = (r16 & 7) >> 1;
    const v4u fid = {dw == 0 ? one : 0u, dw == 1 ? one : 0u, dw == 2 ? one : 0u, dw == 3 ? one : 0u};
    v4u Gf[2][8], Ff[2][8];
#define CV_LOADF(rho, buf) _Pragma("unroll") for (int i = 0; i < 8; ++i) Ff[buf][i] = *(LAS v4u_a8 *)((i < 4 ? f1 : f0) + (7 - (rho)) * 64 + ((7 - i) & 3) * 512);
#define CV_LOADG(rho, buf) _Pragma("unroll") for (int i = 0; i < 8; ++i) Gf[buf][i] = *(LAS v4u *)(gb[(rho) & 3] + ((rho) < 4 ? 2048 : 0) + i * 4096);
    CV_LOADF(0, 0)
    SCHED();
    BARRIER();
    f32x4 acc[8];
#pragma unroll
    for (int J = 0; J < 8; ++J) Gf[1][J] = *(LAS v4u *)(eb + J * 4096);
    CV_LOADG(0, 0)
    SCHED();
#pragma unroll
    for (int J = 0; J < 8; ++J) acc[J] = mfma(fid, Gf[1][J], f32x4{0.f, 0.f, 0.f, 0.f});
    SCHED();
#pragma unroll
    for (int rho = 0; rho < 8; ++rho) {
      if (rho < 7) { CV_LOADG(rho + 1, (rho + 1) & 1) CV_LOADF(rho + 1, (rho + 1) & 1) }
#pragma unroll
      for (int Dd = 0; Dd < 8; ++Dd)
#pragma unroll
        for (int Jp = 0; Jp + Dd < 8; ++Jp) acc[Dd + Jp] = mfma(Ff[rho & 1][Dd], Gf[rho & 1][Jp], acc[Dd + Jp]);
      if (rho < 7) {
#pragma unroll
        for (int k = 0; k < 16; ++k) { SGB(0x8, 2); SGB(0x100, 1); }
        SGB(0x8, 4);
      }
      SCHED();
    }
#undef CV_LOADF
#undef CV_LOADG
#pragma unroll
    for (int k = 0; k < 4; ++k)
      *(LAS v4u *)(lds + CV_X + (w * 4 + k) * 1024 + lane * 16) =
          v4u{pk2(acc[2 * k].x, acc[2 * k].y), pk2(acc[2 * k].z, acc[2 * k].w), pk2(acc[2 * k + 1].x, acc[2 * k + 1].y),
              pk2(acc[2 * k + 1].z, acc[2 * k + 1].w)};
    tag = get_tag(lds);
    BARRIER();
    v4u v[4];
#pragma unroll
    for (int c = 0; c < 4; ++c) v[c] = *(LAS v4u *)(lds + CV_X + (c * 4 + w) * 1024 + lane * 16);
    const int ho = ((r16 >> 2) * 160 * 32 + 4 * (r16 & 3) + g) * 16;
#pragma unroll
    for (int z = 0; z < 4; ++z) {
      const v4u ch = {__builtin_amdgcn_perm(v[1][z], v[0][z], 0x05040100u), __builtin_amdgcn_perm(v[3][z], v[2][z], 0x05040100u),
                      __builtin_amdgcn_perm(v[1][z], v[0][z], 0x07060302u), __builtin_amdgcn_perm(v[3][z], v[2][z], 0x07060302u)};
      st16(rs, ho, OFF_H + ((4 * (2 * w + (z >> 1)) * 160 + c4) * 32 + (z & 1) * 16) * 16, ch);
    }
    publish(rs, F_CV + (w * 160 + c4) * 8, tag, lane);
  } else {
    const int unit = w * 96 + (idx - 20) * 8 + xg;
    const u16 *W; int N, strip, ks0, nks = 5, KS = 20, dst, per = 5;
    if (unit < 40) { W = p.w1; N = D; strip = unit >> 2; ks0 = (unit & 3) * 5; dst = OFF_WT1; }
    else if (unit < 80) { W = p.w2; N = D; strip = (unit - 40) >> 2; ks0 = (unit & 3) * 5; dst = OFF_WT2; }
    else if (unit < 176) { W = p.w3; N = F; strip = (unit - 80) >> 2; ks0 = (unit & 3) * 5; dst = OFF_WT3; per = 6; }
    else { W = p.w4; N = D; strip = (unit - 176) / 12; ks0 = ((unit - 176) % 12) * 4; nks = 4; KS = 48; dst = OFF_WT4; }
    if (unit >= 296) nks = 0;
    LAS unsigned char *my = lds + w * 20480;
    {
      const int r8 = lane >> 3, cpos = lane & 7, csrc = cpos ^ (2 * ((r8 >> 1) & 3));
      const unsigned char *src = (const unsigned char *)(W + (size_t)(ks0 * 32 + r8) * N + strip * 64) + csrc * 16;
      BARRIER();
      for (int i = 0; i < nks * 4; ++i) dma1(src + (size_t)i * 8 * N * 2, lds_addr(my) + i * 1024);
    }
    DRAIN();
    const int qq = (lane >> 2) & 3, pp = lane & 3, row = 4 * g + qq;
    for (int s = 0; s < nks; ++s) {
#pragma unroll
      for (int t = 0; t < 4; ++t) {
        const int chunk = (2 * t + (pp >> 1)) ^ (2 * ((row >> 1) & 3));
        LAS unsigned char *src = my + (s * 32 + row) * 128 + chunk * 16 + (pp & 1) * 8;
        const v2u a = tr_read(src), b = tr_read(src + 16 * 128);
        const int nt = strip * 4 + t;
        const int j = nt / per, rem = nt - j * per;
        const int i = (per == 5 && (j & 1)) ? (rem == 0 ? 4 : rem - 1) : rem;
        st16(rs, lane * 16, dst + ((j * KS + ks0 + s) * per + i) * 1024, v4u{a.x, a.y, b.x, b.y});
      }
    }
    tag = get_tag(lds);
    if (nks) publish(rs, F_W + unit * 8, tag, lane);
    BARRIER();
    BARRIER();
  }
  BARRIER();

  const int rt = 4 * xg + (idx >> 3), j = idx & 7, ps = w & 1, kh = w >> 1, q = 2 * ps + kh;
  const int tq = rt * 64 + 4 * r16 + q;
  LAS unsigned char *xo = lds + G_X + w * 5120 + lane * 16, *xi = lds + G_X + (w ^ 2) * 5120 + lane * 16;
  float mean1, rstd1;
  {
    f32x4 cs[5];
#pragma unroll
    for (int i = 0; i < 5; ++i) {
      LAS f32x4 *c = (LAS f32x4 *)(lds + G_CS + i * 64 + g * 16);
      cs[i] = (c[0] + c[24]) + (c[48] + c[72]);
    }
    LAS unsigned char *wb = lds + kh * 50 * 1024 + lane * 16;
    v4u wf[2][5];
#pragma unroll
    for (int i = 0; i < 5; ++i) wf[0][i] = *(LAS v4u *)(wb + i * 1024);
    const int av = (g * 32 + r16) * 16, as = OFF_H + ((rt * 160 + kh * 80) * 32 + ps * 16) * 16;
    wait16<1>(rs, F_CV + ((rt >> 3) * 160 + kh * 80 + 2 * (lane < 40 ? lane : 0)) * 8, tag);
    constexpr int WIN = 4;
    v4u la[WIN], lb[WIN];
#pragma unroll
    for (int s = 0; s < WIN; ++s) { la[s] = ld16(rs, av, as + s * 4096); lb[s] = ld16(rs, av + 2048, as + s * 4096); }
    f32x4 acc[2][5];
#pragma unroll
    for (int i = 0; i < 5; ++i) { acc[0][i] = f32x4{0.f, 0.f, 0.f, 0.f}; acc[1][i] = f32x4{0.f, 0.f, 0.f, 0.f}; }
    float s0 = 0.f, q0 = 0.f, s1 = 0.f, q1 = 0.f;
    SCHED();
#pragma unroll
    for (int s = 0; s < 10; ++s) {
      const v4u a = la[s % WIN], b = lb[s % WIN];
      const v4u fa = {a.x, a.y, b.x, b.y}, fb = {a.z, a.w, b.z, b.w};
      if (s + WIN < 10) { la[s % WIN] = ld16(rs, av, as + (s + WIN) * 4096); lb[s % WIN] = ld16(rs, av + 2048, as + (s + WIN) * 4096); }
      if (s < 9) {
#pragma unroll
        for (int i = 0; i < 5; ++i) wf[(s + 1) & 1][i] = *(LAS v4u *)(wb + ((s + 1) * 5 + i) * 1024);
      }
#pragma unroll
      for (int i = 0; i < 5; ++i) {
        acc[0][i] = mfma(wf[s & 1][i], fa, acc[0][i]);
        acc[1][i] = mfma(wf[s & 1][i], fb, acc[1][i]);
      }
#pragma unroll
      for (int e = 0; e < 4; ++e) {
        s0 = dot2(fa[e], ONE2, s0); q0 = dot2(fa[e], fa[e], q0);
        s1 = dot2(fb[e], ONE2, s1); q1 = dot2(fb[e], fb[e], q1);
      }
      SGB(0x2, 8);
      SGB(0x20, 2);
#pragma unroll
      for (int k = 0; k < 5; ++k) { SGB(0x8, 1); SGB(0x100, 1); SGB(0x2, 1); }
#pragma unroll
      for (int k = 0; k < 5; ++k) { SGB(0x8, 1); SGB(0x2, 2); }
      SCHED();
    }
    *(LAS f32x2 *)(lds + G_S + (((2 * ps) * 16 + r16) * 8 + kh * 4 + g) * 8) = f32x2{s0, q0};
    *(LAS f32x2 *)(lds + G_S + (((2 * ps + 1) * 16 + r16) * 8 + kh * 4 + g) * 8) = f32x2{s1, q1};
    if (kh) {
#pragma unroll
      for (int i = 0; i < 5; ++i) *(LAS f32x4 *)(xo + i * 1024) = acc[0][i];
    } else {
#pragma unroll
      for (int i = 0; i < 5; ++i) *(LAS f32x4 *)(xo + i * 1024) = acc[1][i];
    }
    BARRIER();
    if (bid == 0 && w == 0 && lane == 0) st8(rs, 0, 0, tag);
    LAS f32x4 *sp = (LAS f32x4 *)(lds + G_S + (q * 16 + r16) * 64);
    const f32x4 st = (sp[0] + sp[1]) + (sp[2] + sp[3]);
    f32x4 v[5];
    if (kh) {
#pragma unroll
      for (int i = 0; i < 5; ++i) v[i] = acc[1][i] + *(LAS f32x4 *)(xi + i * 1024);
    } else {
#pragma unroll
      for (int i = 0; i < 5; ++i) v[i] = acc[0][i] + *(LAS f32x4 *)(xi + i * 1024);
    }
    mean1 = (st.x + st.z) * (1.f / D);
    const float var = fmaxf((st.y + st.w) * (1.f / D) - mean1 * mean1, 0.f);
    rstd1 = __builtin_amdgcn_rsqf(var + EPS);
    const float r2 = __builtin_amdgcn_rsqf(var * rstd1 * rstd1 + EPS);
    const float rr = rstd1 * r2, nm = -mean1 * rr;
    const int td = tq + shift_of(j);
    const bool oob = (unsigned)td >= (unsigned)L;
    v2u pk[5];
#pragma unroll
    for (int i = 0; i < 5; ++i) {
      const f32x4 pre = v[i] * rr + cs[i] * nm;
      const f32x2 a = gelu2(f32x2{pre.x, pre.y}), b = gelu2(f32x2{pre.z, pre.w});
      pk[i] = v2u{oob ? 0u : pk2(a), oob ? 0u : pk2(b)};
    }
    store_frag5(rs, OFF_H1, td & (L - 1), j, g, pk);
    publish(rs, F_G1 + ((rt * 4 + q) * 8 + j) * 8, tag, lane);
  }
  BARRIER();
  const int hoff = (g * 32 + (q >> 1) * 16 + r16) * 16 + (q & 1) * 8;
  {
    v2u hv[5];
#pragma unroll
    for (int i = 0; i < 5; ++i) hv[i] = ld8(rs, hoff, OFF_H + (rt * 160 + ntile5(j, i) * 4) * 512);
    LAS unsigned char *wb = lds + kh * 50 * 1024 + lane * 16;
    v4u wf[2][5];
#pragma unroll
    for (int i = 0; i < 5; ++i) wf[0][i] = *(LAS v4u *)(wb + i * 1024);
    {
      const int jj = 4 * kh + (lane & 3), qd = 2 * ps + ((lane >> 2) & 1), sh = shift_of(jj);
      const int qs = (qd - sh) & 3, carry = (qs + sh) >> 2;
      const int rs_ = (rt - ((lane & 8) ? carry : 0)) & 31;
      wait8<1>(rs, F_G1 + ((rs_ * 4 + qs) * 8 + jj) * 8, tag);
    }
    const int as = OFF_H1 + (rt * 20 + kh * 10) * 4096;
    const int ava = q * 1024 + lane * 16, avb = (q ^ 1) * 1024 + lane * 16;
    constexpr int WIN = 4;
    v4u la[WIN], lb[WIN];
#pragma unroll
    for (int s = 0; s < WIN; ++s) { la[s] = ld16(rs, ava, as + s * 4096); lb[s] = ld16(rs, avb, as + s * 4096); }
    f32x4 acc[2][5];
#pragma unroll
    for (int i = 0; i < 5; ++i) { acc[0][i] = f32x4{0.f, 0.f, 0.f, 0.f}; acc[1][i] = f32x4{0.f, 0.f, 0.f, 0.f}; }
    SCHED();
#pragma unroll
    for (int s = 0; s < 10; ++s) {
      if (s < 9) {
#pragma unroll
        for (int i = 0; i < 5; ++i) wf[(s + 1) & 1][i] = *(LAS v4u *)(wb + ((s + 1) * 5 + i) * 1024);
      }
#pragma unroll
      for (int i = 0; i < 5; ++i) acc[0][i] = mfma(wf[s & 1][i], la[s % WIN], acc[0][i]);
      if (s + WIN < 10) la[s % WIN] = ld16(rs, ava, as + (s + WIN) * 4096);
#pragma unroll
      for (int i = 0; i < 5; ++i) acc[1][i] = mfma(wf[s & 1][i], lb[s % WIN], acc[1][i]);
      if (s + WIN < 10) lb[s % WIN] = ld16(rs, avb, as + (s + WIN) * 4096);
#pragma unroll
      for (int k = 0; k < 5; ++k) { SGB(0x8, 1); SGB(0x100, 1); }
      SGB(0x20, 1);
      SGB(0x8, 5);
      SGB(0x20, 1);
      SCHED();
    }
#pragma unroll
    for (int i = 0; i < 5; ++i) *(LAS f32x4 *)(xo + i * 1024) = acc[1][i];
    BARRIER();
    v2u pk[5];
    const float nm = -mean1 * rstd1;
#pragma unroll
    for (int i = 0; i < 5; ++i) {
      const f32x4 hs = acc[0][i] + *(LAS f32x4 *)(xi + i * 1024);
      const f32x4 h4 = {bflo(hv[i].x), bfhi(hv[i].x), bflo(hv[i].y), bfhi(hv[i].y)};
      const f32x4 x1 = (h4 * rstd1 + nm) + hs;
      pk[i] = v2u{pk2(x1.x, x1.y), pk2(x1.z, x1.w)};
    }
    store_frag5(rs, OFF_X1, tq, j, g, pk);
    publish(rs, F_G2 + ((rt * 4 + q) * 8 + j) * 8, tag, lane);
  }
  BARRIER();
  {
    const int j3 = idx & 15, rt3 = 4 * xg + 2 * (idx >> 4) + (w >> 1), q0 = 2 * (w & 1);
    f32x4 cs[6];
#pragma unroll
    for (int i = 0; i < 6; ++i) {
      LAS f32x4 *c = (LAS f32x4 *)(lds + G_CS + i * 64 + g * 16);
      cs[i] = (c[0] + c[24]) + (c[48] + c[72]);
    }
    LAS unsigned char *wb = lds + lane * 16;
    v4u wf[2][6];
#pragma unroll
    for (int i = 0; i < 6; ++i) wf[0][i] = *(LAS v4u *)(wb + i * 1024);
    wait8<1>(rs, F_G2 + ((rt3 * 4 + q0) * 8 + (lane & 15)) * 8, tag);
    const int as = OFF_X1 + (rt3 * 80 + q0) * 1024, av = lane * 16;
    constexpr int WIN = 5;
    v4u la[WIN], lb[WIN];
#pragma unroll
    for (int s = 0; s < WIN; ++s) { la[s] = ld16(rs, av, as + s * 4096); lb[s] = ld16(rs, av + 1024, as + s * 4096); }
    f32x4 acc[2][6];
#pragma unroll
    for (int i = 0; i < 6; ++i) { acc[0][i] = f32x4{0.f, 0.f, 0.f, 0.f}; acc[1][i] = f32x4{0.f, 0.f, 0.f, 0.f}; }
    float s0 = 0.f, qq0 = 0.f, s1 = 0.f, qq1 = 0.f;
    SCHED();
#pragma unroll
    for (int s = 0; s < 20; ++s) {
      if (s < 19) {
#pragma unroll
        for (int i = 0; i < 6; ++i) wf[(s + 1) & 1][i] = *(LAS v4u *)(wb + ((s + 1) * 6 + i) * 1024);
      }
      const v4u a = la[s % WIN], b = lb[s % WIN];
#pragma unroll
      for (int i = 0; i < 6; ++i) acc[0][i] = mfma(wf[s & 1][i], a, acc[0][i]);
#pragma unroll
      for (int e = 0; e < 4; ++e) { s0 = dot2(a[e], ONE2, s0); qq0 = dot2(a[e], a[e], qq0); }
      if (s + WIN < 20) la[s % WIN] = ld16(rs, av, as + (s + WIN) * 4096);
#pragma unroll
      for (int i = 0; i < 6; ++i) acc[1][i] = mfma(wf[s & 1][i], b, acc[1][i]);
#pragma unroll
      for (int e = 0; e < 4; ++e) { s1 = dot2(b[e], ONE2, s1); qq1 = dot2(b[e], b[e], qq1); }
      if (s + WIN < 20) lb[s % WIN] = ld16(rs, av + 1024, as + (s + WIN) * 4096);
#pragma unroll
      for (int k = 0; k < 4; ++k) { SGB(0x8, 1); SGB(0x2, 2); }
      SGB(0x8, 1); SGB(0x100, 2); SGB(0x8, 1); SGB(0x100, 1); SGB(0x20, 1);
#pragma unroll
      for (int k = 0; k < 4; ++k) { SGB(0x8, 1); SGB(0x2, 2); }
      SGB(0x8, 1); SGB(0x100, 2); SGB(0x8, 1); SGB(0x100, 1); SGB(0x20, 1);
      SCHED();
    }
    BARRIER();
    s0 = sum_x32(sum_x16(s0)); qq0 = sum_x32(sum_x16(qq0)); s1 = sum_x32(sum_x16(s1)); qq1 = sum_x32(sum_x16(qq1));
#pragma unroll
    for (int m = 0; m < 2; ++m) {
      const float mean = (m ? s1 : s0) * (1.f / D);
      const float var = fmaxf((m ? qq1 : qq0) * (1.f / D) - mean * mean, 0.f);
      const float rr = __builtin_amdgcn_rsqf(var + EPS), nm = -mean * rr;
#pragma unroll
      for (int k = 0; k < 3; ++k) {
        const f32x4 pa = acc[m][2 * k] * rr + cs[2 * k] * nm, pb = acc[m][2 * k + 1] * rr + cs[2 * k + 1] * nm;
        st16(rs, lane * 16, OFF_HID + ((rt3 * 48 + j3 * 3 + k) * 4 + q0 + m) * 1024,
             v4u{pk2(gelu2(f32x2{pa.x, pa.y})), pk2(gelu2(f32x2{pa.z, pa.w})), pk2(gelu2(f32x2{pb.x, pb.y})), pk2(gelu2(f32x2{pb.z, pb.w}))});
      }
    }
    publish(rs, F_G3 + ((rt3 * 2 + (w & 1)) * 16 + j3) * 8, tag, lane);
  }
  BARRIER();
  {
    v2u hv[5], xv[5];
#pragma unroll
    for (int i = 0; i < 5; ++i) {
      const int nt = ntile5(j, i);
      hv[i] = ld8(rs, hoff, OFF_H + (rt * 160 + nt * 4) * 512);
      xv[i] = ld8(rs, q * 1024 + lane * 16, OFF_X1 + (rt * 20 + (nt >> 1)) * 4096 + (nt & 1) * 8);
    }
    LAS unsigned char *wb = lds + kh * 70 * 1024 + lane * 16;
    v4u wf[2][5];
#pragma unroll
    for (int i = 0; i < 5; ++i) wf[0][i] = *(LAS v4u *)(wb + i * 1024);
    wait8<1>(rs, F_G3 + ((rt * 2 + ps) * 16 + 8 * kh + (lane & 7)) * 8, tag);
    const int as = OFF_HID + (rt * 48 + kh * 24) * 4096;
    const int ava = q * 1024 + lane * 16, avb = (q ^ 1) * 1024 + lane * 16;
    constexpr int WIN = 6;
    v4u la[WIN], lb[WIN];
#pragma unroll
    for (int s = 0; s < WIN; ++s) { la[s] = ld16(rs, ava, as + s * 4096); lb[s] = ld16(rs, avb, as + s * 4096); }
    f32x4 acc[2][5];
#pragma unroll
    for (int i = 0; i < 5; ++i) { acc[0][i] = f32x4{0.f, 0.f, 0.f, 0.f}; acc[1][i] = f32x4{0.f, 0.f, 0.f, 0.f}; }
    SCHED();
#pragma unroll
    for (int s = 0; s < 24; ++s) {
      if (s == 3 || s == 9 || s == 17) { BARRIER(); SCHED(); }
      if (s < 23) {
        const int slot = s + 1 < 14 ? s + 1 : s + 1 - 14;
#pragma unroll
        for (int i = 0; i < 5; ++i) wf[(s + 1) & 1][i] = *(LAS v4u *)(wb + (slot * 5 + i) * 1024);
      }
#pragma unroll
      for (int i = 0; i < 5; ++i) acc[0][i] = mfma(wf[s & 1][i], la[s % WIN], acc[0][i]);
      if (s + WIN < 24) la[s % WIN] = ld16(rs, ava, as + (s + WIN) * 4096);
#pragma unroll
      for (int i = 0; i < 5; ++i) acc[1][i] = mfma(wf[s & 1][i], lb[s % WIN], acc[1][i]);
      if (s + WIN < 24) lb[s % WIN] = ld16(rs, avb, as + (s + WIN) * 4096);
#pragma unroll
      for (int k = 0; k < 5; ++k) { SGB(0x8, 1); SGB(0x100, 1); }
      SGB(0x20, 1);
      SGB(0x8, 5);
      SGB(0x20, 1);
      SCHED();
    }
    LAS unsigned char *xo4 = lds + G4_X + w * 5120 + lane * 16, *xi4 = lds + G4_X + (w ^ 2) * 5120 + lane * 16;
#pragma unroll
    for (int i = 0; i < 5; ++i) *(LAS f32x4 *)(xo4 + i * 1024) = acc[1][i];
    BARRIER();
    u16 *orow = p.out + (size_t)tq * D + 4 * g;
#pragma unroll
    for (int i = 0; i < 5; ++i) {
      const f32x4 h2 = acc[0][i] + *(LAS f32x4 *)(xi4 + i * 1024);
      const f32x4 h4 = {bflo(hv[i].x), bfhi(hv[i].x), bflo(hv[i].y), bfhi(hv[i].y)};
      const f32x4 x4 = {bflo(xv[i].x), bfhi(xv[i].x), bflo(xv[i].y), bfhi(xv[i].y)};
      const f32x4 o = h4 + (x4 + h2);
      *(GAS v2u *)(orow + ntile5(j, i) * 16) = v2u{pk2(o.x, o.y), pk2(o.z, o.w)};
    }
  }
}

template <int N, int PER> __device__ __forceinline__ void colsum_store(LAS unsigned char *lds, int x, int lane, const f32x4 (&c)[PER]) {
  if ((lane & 15) == 0) {
#pragma unroll
    for (int i = 0; i < PER; ++i) *(LAS f32x4 *)(lds + G_CS + x * 384 + i * 64 + (lane >> 4) * 16) = c[i];
  }
}
__device__ __forceinline__ void role_aux(const Params &p, LAS unsigned char *lds, rsrc_t rs, int x, int lane) {
  v2u tag;
  const int bid = blockIdx.x, xg = bid & 7, idx = bid >> 3;
  const bool conv = idx < 20;
  {
    const int theta = bid, tok = x * 2 + (lane >> 5), l32 = lane & 31;
    const GAS v2u *xr = (const GAS v2u *)(p.x + (size_t)(theta * 8 + tok) * D);
    v2u xv[5], dv[5];
#pragma unroll
    for (int i = 0; i < 5; ++i) xv[i] = xr[l32 + 32 * i];
#pragma unroll
    for (int i = 0; i < 5; ++i) dv[i] = ((const GAS v2u *)p.Dsk)[l32 + 32 * i];
    const v2u ep = ld8(rs, 0, 0);
    if (x == 0 && lane == 0) *(LAS unsigned *)(lds + LN_CNT) = 0u;
    SCHED();
    BARRIER();
    float s = 0.f, q = 0.f;
#pragma unroll
    for (int i = 0; i < 5; ++i) {
      s = dot2(xv[i].x, ONE2, s); s = dot2(xv[i].y, ONE2, s);
      q = dot2(xv[i].x, xv[i].x, q); q = dot2(xv[i].y, xv[i].y, q);
    }
    s = sum_x16(sum16(s)); q = sum_x16(sum16(q));
    const float mean = s * (1.f / D);
    const float var = fmaxf(q * (1.f / D) - mean * mean, 0.f);
    const float rstd = __builtin_amdgcn_rsqf(var + EPS);
    const f32x2 rs2 = {rstd, rstd}, mb2 = {-mean * rstd, -mean * rstd};
    LAS unsigned char *st = lds + LN_ST;
#pragma unroll
    for (int i = 0; i < 5; ++i) {
      f32x2 x0 = {bflo(xv[i].x), bfhi(xv[i].x)}, x1 = {bflo(xv[i].y), bfhi(xv[i].y)};
      v2u u = {pk2(__builtin_elementwise_fma(x0, rs2, mb2)), pk2(__builtin_elementwise_fma(x1, rs2, mb2))};
      f32x2 u0 = {bflo(u.x), bfhi(u.x)}, u1 = {bflo(u.y), bfhi(u.y)};
      f32x2 d0 = {bflo(dv[i].x), bfhi(dv[i].x)}, d1 = {bflo(dv[i].y), bfhi(dv[i].y)};
      v2u e = {pk2(__builtin_elementwise_fma(u0, d0, x0)), pk2(__builtin_elementwise_fma(u1, d1, x1))};
      *(LAS v2u *)(st + tok * LN_PITCH + (l32 + 32 * i) * 8) = u;
      *(LAS v2u *)(st + (8 + tok) * LN_PITCH + (l32 + 32 * i) * 8) = e;
    }
    {
      const u64 e1 = (((u64)(unsigned)__builtin_amdgcn_readfirstlane(ep.y) << 32) | (unsigned)__builtin_amdgcn_readfirstlane(ep.x)) + 1ull;
      tag = v2u{(unsigned)e1, (unsigned)(e1 >> 32)};
      if (x == 0 && lane == 0) *(LAS v2u *)(lds + LN_CNT + 8) = tag;
    }
    asm volatile("s_waitcnt lgkmcnt(0)" ::: "memory");
    if (lane == 0) __hip_atomic_fetch_add((LAS unsigned *)(lds + LN_CNT), 1u, __ATOMIC_RELAXED, __HIP_MEMORY_SCOPE_WORKGROUP);
#pragma unroll 1
    for (unsigned spins = 0; spins < SPIN_CAP; ++spins)
      if (__hip_atomic_load((LAS unsigned *)(lds + LN_CNT), __ATOMIC_RELAXED, __HIP_MEMORY_SCOPE_WORKGROUP) >= 4u) break;
    asm volatile("" ::: "memory");
    const int G = lane >> 4, qq = (lane >> 2) & 3, pp = lane & 3, li = lane & 15;
    const int so = ((2 * G + (li >> 3)) * 256) * 128 + (((li & 7) + (theta >> 1)) & 7) * 16;
#pragma unroll
    for (int it = 0; it < 5; ++it) {
      const int cb = (x * 5 + it) * 8;
      LAS unsigned char *src = st + ((pp & 1) * 8 + qq) * LN_PITCH + (cb + 2 * G + (pp >> 1)) * 8;
      const v2u a = tr_read(src), b = tr_read(src + 4 * LN_PITCH);
      st16(rs, so, OFF_UE + (cb * 256 + theta) * 128, v4u{a.x, a.y, b.x, b.y});
    }
    publish(rs, F_LN + (x * 256 + theta) * 8, tag, lane);
  }
  if (conv) {
    const int c4 = xg * 20 + idx;
    wait8<1>(rs, F_LN + ((c4 / 40) * 256 + x * 64 + lane) * 8, tag);
    v4u t[8];
#pragma unroll
    for (int i = 0; i < 8; ++i) t[i] = ld16(rs, lane * 16, OFF_UE + c4 * 32768 + x * 8192 + i * 1024);
#pragma unroll
    for (int i = 0; i < 8; ++i) *(LAS v4u *)(lds + CV_UE + 4096 + x * 8192 + i * 1024 + lane * 16) = t[i];
  }
  BARRIER();
  const int j = idx & 7, j3 = idx & 15, lv = lane * 16;
  const int s1 = OFF_WT1 + (j * 100 + x * 25) * 1024, s2 = OFF_WT2 + (j * 100 + x * 25) * 1024, s3 = OFF_WT3 + (j3 * 120 + x * 30) * 1024;
  const int h4 = x >> 1, o4 = x & 1;
  const int s4a = OFF_WT4 + (j * 48 + h4 * 24 + 7 * o4) * 5120, s4b = OFF_WT4 + (j * 48 + h4 * 24 + 14 + 2 * o4) * 5120,
            s4c = OFF_WT4 + (j * 48 + h4 * 24 + 18 + 3 * o4) * 5120;
#define W4SRC(n) ((n) < 35 ? s4a + (n) * 1024 : (n) < 45 ? s4b + ((n) - 35) * 1024 : s4c + ((n) - 45) * 1024)
  const v4u ones = {ONE2, ONE2, ONE2, ONE2};
  v4u A[25], B[25], C[6];
  wait16<8>(rs, F_W + 2 * (lane < 40 ? lane : 0) * 8, tag);
#pragma unroll
  for (int i = 0; i < 25; ++i) A[i] = ld16(rs, lv, s1 + i * 1024);
#pragma unroll
  for (int i = 0; i < 25; ++i) B[i] = ld16(rs, lv, s2 + i * 1024);
  SCHED();
  wait16<8>(rs, F_W + (80 + 2 * (lane < 48 ? lane : 0)) * 8, tag);
  wait16<8>(rs, F_W + (176 + 2 * (lane < 60 ? lane : 0)) * 8, tag);
#pragma unroll
  for (int i = 0; i < 6; ++i) C[i] = ld16(rs, lv, s3 + i * 1024);
  SCHED();
  BARRIER();
  {
    f32x4 c[5];
#pragma unroll
    for (int i = 0; i < 5; ++i) c[i] = f32x4{0.f, 0.f, 0.f, 0.f};
#pragma unroll
    for (int i = 0; i < 25; ++i) {
      *(LAS v4u *)(lds + (x * 25 + i) * 1024 + lv) = A[i];
      c[i % 5] = mfma(A[i], ones, c[i % 5]);
    }
    colsum_store<25, 5>(lds, x, lane, c);
  }
  BARRIER();
#pragma unroll
  for (int i = 0; i < 24; ++i) A[i] = ld16(rs, lv, s3 + (6 + i) * 1024);
  A[24] = ld16(rs, lv, W4SRC(0));
  SCHED();
  BARRIER();
#pragma unroll
  for (int i = 0; i < 25; ++i) *(LAS v4u *)(lds + (x * 25 + i) * 1024 + lv) = B[i];
  BARRIER();
#pragma unroll
  for (int i = 0; i < 25; ++i) B[i] = ld16(rs, lv, W4SRC(1 + i));
  SCHED();
  BARRIER();
  {
    f32x4 c[6];
#pragma unroll
    for (int i = 0; i < 6; ++i) c[i] = f32x4{0.f, 0.f, 0.f, 0.f};
#pragma unroll
    for (int i = 0; i < 30; ++i) {
      const v4u f = i < 6 ? C[i < 6 ? i : 0] : A[i < 6 ? 0 : i - 6];
      *(LAS v4u *)(lds + (x * 30 + i) * 1024 + lv) = f;
      c[i % 6] = mfma(f, ones, c[i % 6]);
    }
    colsum_store<30, 6>(lds, x, lane, c);
  }
  BARRIER();
#pragma unroll
  for (int i = 0; i < 24; ++i) A[i] = ld16(rs, lv, W4SRC(26 + i));
#pragma unroll
  for (int i = 0; i < 6; ++i) C[i] = ld16(rs, lv, W4SRC(50 + i));
  SCHED();
  BARRIER();
  {
    LAS unsigned char *o = lds + (h4 * 14 + 7 * o4) * 5120 + lv;
    *(LAS v4u *)(o) = A[24];
#pragma unroll
    for (int i = 0; i < 25; ++i) *(LAS v4u *)(o + (1 + i) * 1024) = B[i];
#pragma unroll
    for (int i = 0; i < 9; ++i) *(LAS v4u *)(o + (26 + i) * 1024) = A[i];
#pragma unroll
    for (int i = 0; i < 4; ++i) B[i] = ld16(rs, lv, W4SRC(56 + i));
  }
  BARRIER();
  BARRIER();
  {
    LAS unsigned char *o = lds + (h4 * 14 + 2 * o4) * 5120 + lv;
#pragma unroll
    for (int i = 0; i < 10; ++i) *(LAS v4u *)(o + i * 1024) = A[9 + i];
  }
  BARRIER();
  {
    LAS unsigned char *o = lds + (h4 * 14 + 4 + 3 * o4) * 5120 + lv;
#pragma unroll
    for (int i = 0; i < 5; ++i) *(LAS v4u *)(o + i * 1024) = A[19 + i];
#pragma unroll
    for (int i = 0; i < 6; ++i) *(LAS v4u *)(o + (5 + i) * 1024) = C[i];
#pragma unroll
    for (int i = 0; i < 4; ++i) *(LAS v4u *)(o + (11 + i) * 1024) = B[i];
  }
  BARRIER();
  BARRIER();
#undef W4SRC
}

__global__ void __launch_bounds__(512, 2) k_fused(Params p) {
  __shared__ __attribute__((aligned(16))) unsigned char smem[LDS_BYTES];
  LAS unsigned char *lds = (LAS unsigned char *)smem;
  const int lane = threadIdx.x & 63, w = __builtin_amdgcn_readfirstlane(threadIdx.x >> 6);
  const rsrc_t rs = __builtin_amdgcn_make_buffer_rsrc((void *)p.ws, 0, WS_BYTES, 0x00020000);
  if (w < 4) role_compute(p, lds, rs, w, lane);
  else role_aux(p, lds, rs, w - 4, lane);
}

extern "C" void kernel_launch(void *const *d_in, const int *in_sizes, int n_in, void *d_out, int out_size, void *d_ws,
                              size_t ws_size, hipStream_t stream) {
  Params p;
  p.x = (const u16 *)d_in[0]; p.Are = (const u16 *)d_in[1]; p.Aim = (const u16 *)d_in[2]; p.Bre = (const u16 *)d_in[3];
  p.Bim = (const u16 *)d_in[4]; p.Cre = (const u16 *)d_in[5]; p.Cim = (const u16 *)d_in[6]; p.logdt = (const u16 *)d_in[7];
  p.Dsk = (const u16 *)d_in[8]; p.w1 = (const u16 *)d_in[10]; p.w2 = (const u16 *)d_in[11]; p.w3 = (const u16 *)d_in[12];
  p.w4 = (const u16 *)d_in[13]; p.out = (u16 *)d_out; p.ws = (unsigned char *)d_ws;
  k_fused<<<256, 512, 0, stream>>>(p);
}
```

```cpp
#include <hip/hip_runtime.h>
#include <hip/hip_bf16.h>
#include <stdint.h>

#define GAS __attribute__((address_space(1)))
#define LAS __attribute__((address_space(3)))
typedef unsigned short u16;
typedef unsigned long long u64;
typedef unsigned v4u __attribute__((ext_vector_type(4)));
typedef v4u v4u_a8 __attribute__((aligned(8)));
typedef unsigned v2u __attribute__((ext_vector_type(2)));
typedef float f32x4 __attribute__((ext_vector_type(4)));
typedef float f32x2 __attribute__((ext_vector_type(2)));
typedef short bf16x8 __attribute__((ext_vector_type(8)));
typedef short s16x4 __attribute__((ext_vector_type(4)));
typedef __bf16 bf16x2 __attribute__((ext_vector_type(2)));
typedef _Float16 f16x2 __attribute__((ext_vector_type(2)));
typedef _Float16 f16x8 __attribute__((ext_vector_type(8)));
typedef __amdgpu_buffer_rsrc_t rsrc_t;

constexpr int L = 2048, D = 640, NM = 192, F = 1536;
constexpr float EPS = 1e-5f;
constexpr int G12_PRE = 6, G3_PRE = 4, G3_WIN = 10, G4_PRE = 4, G4_WIN = 8, XS_AT = 11, MIDB = 12;

constexpr int F_ST = 1024, C_S1 = F_ST + 16384, C_S2 = C_S1 + 256 * 64, C_S3 = C_S2 + 128 * 64, C_S4 = C_S3 + 64 * 64;
constexpr int NCNT = 576;
constexpr int OFF_H = 65536;
constexpr int OFF_H1 = OFF_H + 32 * 160 * 512;
constexpr int OFF_X1 = OFF_H1 + 32 * 20 * 4096;
constexpr int OFF_HID = OFF_X1 + 32 * 20 * 4096;
constexpr int OFF_XS = OFF_HID + 32 * 48 * 4096;
constexpr int OFF_WT3 = OFF_XS + 2048 * 64;
constexpr int OFF_WT4 = OFF_WT3 + 1920 * 1024;
constexpr int OFF_WT1 = OFF_WT4 + 1920 * 1024;
constexpr int OFF_WT2 = OFF_WT1 + 800 * 1024;
constexpr int F_W = OFF_WT2 + 800 * 1024;
constexpr int F_INIT = F_W + 680 * 64;
constexpr int WS_BYTES = F_INIT + 32 * 64;

constexpr int CV_UE = 0, CV_KC = 36864, CV_KCW = 16640, CV_KP = 4128, CV_CMP = CV_KC + 4 * CV_KCW, CV_CMPW = 2304, CV_RF = 1088 + 256, CV_RFP = 576;
constexpr int CV_X = 112 * 1024;
constexpr int FP = 1056;
constexpr int G_X = 124 * 1024, G_S = 144 * 1024, G_CS = 148 * 1024, L_TAG = 151 * 1024, G4_X = 50 * 1024;
constexpr int LDS_BYTES = 160 * 1024;

#define BID ((int)((blockIdx.x + 160) & 255))
struct Params {
  const u16 *x, *Are, *Aim, *Bre, *Bim, *Cre, *Cim, *logdt, *Dsk, *w1, *w2, *w3, *w4;
  u16 *out;
  unsigned char *ws;
};

__device__ __forceinline__ float bflo(unsigned v) { return __builtin_bit_cast(float, v << 16); }
__device__ __forceinline__ float bfhi(unsigned v) { return __builtin_bit_cast(float, v & 0xffff0000u); }
__device__ __forceinline__ unsigned pk2(float a, float b) {
  return __builtin_bit_cast(unsigned, __builtin_convertvector(f32x2{a, b}, bf16x2));
}
__device__ __forceinline__ unsigned pk2(f32x2 a) { return __builtin_bit_cast(unsigned, __builtin_convertvector(a, bf16x2)); }
__device__ __forceinline__ f32x4 mfma(v4u a, v4u b, f32x4 c) {
  return __builtin_amdgcn_mfma_f32_16x16x32_bf16(__builtin_bit_cast(bf16x8, a), __builtin_bit_cast(bf16x8, b), c, 0, 0, 0);
}
__device__ __forceinline__ float dot2(unsigned a, unsigned b, float c) {
  return __builtin_amdgcn_fdot2_f32_bf16(__builtin_bit_cast(bf16x2, a), __builtin_bit_cast(bf16x2, b), c, false);
}
extern "C" __device__ float mul1(float, float) __asm("llvm.amdgcn.fmul.legacy");
__device__ __forceinline__ f32x2 cmul(f32x2 x, f32x2 y) {
  f32x2 t = f32x2{x.x, x.x} * y;
  return __builtin_elementwise_fma(f32x2{x.y, x.y}, f32x2{-y.y, y.x}, t);
}
__device__ __forceinline__ f32x2 cm1(f32x2 x, f32x2 y) {
  f32x2 t;
  asm("v_pk_mul_f32 %0, %1, %2 op_sel_hi:[0,1]" : "=v"(t) : "v"(x), "v"(y));
  return t;
}
__device__ __forceinline__ f32x2 cm2(f32x2 x, f32x2 y, f32x2 t) {
  asm("v_pk_fma_f32 %0, %1, %2, %0 op_sel:[1,1,0] op_sel_hi:[1,0,1] neg_lo:[0,1,0]" : "+v"(t) : "v"(x), "v"(y));
  return t;
}
template <int N> __device__ __forceinline__ void cmulN(f32x2 (&r)[N], const f32x2 (&x)[N], const f32x2 (&y)[N]) {
#pragma unroll
  for (int i = 0; i < N; ++i) r[i] = cm1(x[i], y[i]);
  __builtin_amdgcn_sched_barrier(0);
#pragma unroll
  for (int i = 0; i < N; ++i) r[i] = cm2(x[i], y[i], r[i]);
  __builtin_amdgcn_sched_barrier(0);
}
__device__ __forceinline__ unsigned ph(f32x2 a) { return __builtin_bit_cast(unsigned, __builtin_convertvector(a, f16x2)); }
template <int N> __device__ __forceinline__ void hmulN(unsigned (&r)[N], const unsigned (&x)[N], const unsigned (&y)[N]) {
#pragma unroll
  for (int i = 0; i < N; ++i) asm("v_pk_mul_f16 %0, %1, %2 op_sel_hi:[0,1]" : "=v"(r[i]) : "v"(x[i]), "v"(y[i]));
  __builtin_amdgcn_sched_barrier(0);
#pragma unroll
  for (int i = 0; i < N; ++i) asm("v_pk_fma_f16 %0, %1, %2, %0 op_sel:[1,1,0] op_sel_hi:[1,0,1] neg_lo:[0,1,0]" : "+v"(r[i]) : "v"(x[i]), "v"(y[i]));
  __builtin_amdgcn_sched_barrier(0);
}
__device__ __forceinline__ f32x4 mfma_h(v4u a, v4u b, f32x4 c) {
  return __builtin_amdgcn_mfma_f32_16x16x32_f16(__builtin_bit_cast(f16x8, a), __builtin_bit_cast(f16x8, b), c, 0, 0, 0);
}
template <int CTRL> __device__ __forceinline__ float dpp_max(float x) {
  return fmaxf(x, __builtin_bit_cast(float, __builtin_amdgcn_update_dpp(0, __builtin_bit_cast(int, x), CTRL, 0xf, 0xf, false)));
}
template <int CTRL> __device__ __forceinline__ float dpp_add(float x) {
  return x + __builtin_bit_cast(float, __builtin_amdgcn_update_dpp(0, __builtin_bit_cast(int, x), CTRL, 0xf, 0xf, false));
}
__device__ __forceinline__ float sum_x16(float x) {
  const unsigned a = __builtin_bit_cast(unsigned, x);
  auto s = __builtin_amdgcn_permlane16_swap(a, a, false, false);
  const unsigned s0 = s[0], s1 = s[1];
  return __builtin_bit_cast(float, s0) + __builtin_bit_cast(float, s1);
}
__device__ __forceinline__ float sum_x32(float x) {
  const unsigned a = __builtin_bit_cast(unsigned, x);
  auto s = __builtin_amdgcn_permlane32_swap(a, a, false, false);
  const unsigned s0 = s[0], s1 = s[1];
  return __builtin_bit_cast(float, s0) + __builtin_bit_cast(float, s1);
}
__device__ __forceinline__ v2u tr_read(LAS unsigned char *p) {
  return __builtin_bit_cast(v2u, __builtin_amdgcn_ds_read_tr16_b64_v4i16((LAS s16x4 *)p));
}
__device__ __forceinline__ v4u wfrag(LAS unsigned char *p) {
  const v2u a = tr_read(p), b = tr_read(p + 512);
  return v4u{a.x, a.y, b.x, b.y};
}
#define DRAIN() asm volatile("s_waitcnt vmcnt(0)" ::: "memory")
#define SCHED() __builtin_amdgcn_sched_barrier(0)
#define BARRIER() do { SCHED(); asm volatile("s_waitcnt lgkmcnt(0)\n\ts_barrier" ::: "memory"); SCHED(); } while (0)
#define WAIT_LGKM(n) __builtin_amdgcn_s_waitcnt(0xC07F | ((n) << 8))
#define MF(c, a, b) do { asm volatile("v_mfma_f32_16x16x32_bf16 %0, %1, %2, %0" : "+v"(c) : "v"(a), "v"(b)); SCHED(); } while (0)
#define SGB(mask, n) __builtin_amdgcn_sched_group_barrier(mask, n, 0)
constexpr int SC1 = 16;
__device__ __forceinline__ v4u ld16(rsrc_t rs, int voff, int soff) { return __builtin_amdgcn_raw_buffer_load_b128(rs, voff, soff, SC1); }
__device__ __forceinline__ v2u ld8(rsrc_t rs, int voff, int soff) { return __builtin_amdgcn_raw_buffer_load_b64(rs, voff, soff, SC1); }
__device__ __forceinline__ void st16(rsrc_t rs, int voff, int soff, v4u v) { __builtin_amdgcn_raw_buffer_store_b128(v, rs, voff + soff, 0, SC1); }
__device__ __forceinline__ void st8(rsrc_t rs, int voff, int soff, v2u v) { __builtin_amdgcn_raw_buffer_store_b64(v, rs, voff, soff, SC1); }
__device__ __forceinline__ unsigned lds_addr(LAS unsigned char *p) { return (unsigned)(size_t)p; }


constexpr unsigned SPIN_CAP = 1u << 17;
__device__ __forceinline__ void arrive(unsigned char *ws, int off, bool on) {
  DRAIN();
  if (on) __hip_atomic_fetch_add((GAS u64 *)(ws + off), 1ull, __ATOMIC_RELAXED, __HIP_MEMORY_SCOPE_AGENT);
}
template <int SLEEP> __device__ __forceinline__ void wait_cnt(unsigned char *ws, int off, bool on, u64 want) {
#pragma unroll 1
  for (unsigned spins = 0; spins < SPIN_CAP; ++spins) {
    u64 v = want, z = 0ull;
    if (on) asm volatile("global_atomic_add_x2 %0, %1, %2, %3 sc0 sc1\n\ts_waitcnt vmcnt(0)" : "=&v"(v) : "v"(off), "v"(z), "s"(ws) : "memory");
    if (__all(v == want)) break;
    __builtin_amdgcn_s_sleep(SLEEP);
  }
  asm volatile("" ::: "memory");
}
template <int DLY> __device__ __forceinline__ void wait_cnt_pf(unsigned char *ws, int off, u64 want, int p0, int p1, unsigned &d0, unsigned &d1) {
  u64 v = 0ull, z = 0ull;
  __builtin_amdgcn_s_sleep(DLY);
  asm volatile("s_mov_b64 exec, 1\n\tglobal_atomic_add_x2 %0, %3, %4, %5 sc0 sc1\n\ts_mov_b64 exec, -1\n\t"
               "global_load_dword %1, %6, %5 sc1\n\tglobal_load_dword %2, %7, %5 sc1\n\ts_waitcnt vmcnt(2)"
               : "+&v"(v), "=&v"(d0), "=&v"(d1) : "v"(off), "v"(z), "s"(ws), "v"(p0), "v"(p1) : "memory");
  const u64 u = ((u64)(unsigned)__builtin_amdgcn_readfirstlane((unsigned)(v >> 32)) << 32) | (unsigned)__builtin_amdgcn_readfirstlane((unsigned)v);
  if (u != want) wait_cnt<1>(ws, off, (threadIdx.x & 63) == 0, want);
}
__device__ __forceinline__ u64 uni64(v2u v) {
  return ((u64)(unsigned)__builtin_amdgcn_readfirstlane(v.y) << 32) | (unsigned)__builtin_amdgcn_readfirstlane(v.x);
}
__device__ __forceinline__ v2u split64(u64 v) { return v2u{(unsigned)v, (unsigned)(v >> 32)}; }

__device__ __forceinline__ f32x2 gelu2(f32x2 x) {
  constexpr float c0 = -2.0f * 0.7978845608028654f * 1.4426950408889634f, c1 = c0 * 0.044715f;
  f32x2 t = __builtin_elementwise_fma(x * x, f32x2{c1, c1}, f32x2{c0, c0}) * x;
  f32x2 e = f32x2{__builtin_amdgcn_exp2f(t.x), __builtin_amdgcn_exp2f(t.y)} + f32x2{1.f, 1.f};
  return x * f32x2{__builtin_amdgcn_rcpf(e.x), __builtin_amdgcn_rcpf(e.y)};
}
constexpr unsigned ONE2 = 0x3f803f80u;
__device__ __forceinline__ int ntile5(int j, int i) { return (j & 1) ? (i < 4 ? 5 * j + 1 + i : 5 * j) : 5 * j + i; }
__device__ __forceinline__ int shift_of(int j) { const int grp = j >> 1; return (grp & 1) ? -(1 + (grp >> 1)) : 1 + (grp >> 1); }

__device__ __forceinline__ f32x2 cexp_l(float ar2, float af, float l) {
  const float m = __builtin_amdgcn_exp2f(ar2 * l), ph = __builtin_amdgcn_fractf(af * l);
  return f32x2{m * __builtin_amdgcn_cosf(ph), m * __builtin_amdgcn_sinf(ph)};
}

constexpr int TP = 288, KT_SH = 32 * TP, KT_PV = 8 * KT_SH, KT_PW = 25 * TP, KT_PO = 57344;
#define TW4(m, o, r0, r1, r2, r3, a, b, c, d)                                                                                          \
  asm volatile("s_mov_b32 m0, %0\n\ts_nop 0\n\tds_write_addtid_b32 %1 offset:%5\n\tds_write_addtid_b32 %2 offset:%6\n\t"                     \
               "ds_write_addtid_b32 %3 offset:%7\n\tds_write_addtid_b32 %4 offset:%8" ::"s"(m), "v"(a), "v"(b), "v"(c), "v"(d),            \
               "n"((o) + (r0) * TP), "n"((o) + (r1) * TP), "n"((o) + (r2) * TP), "n"((o) + (r3) * TP) : "memory")
#define TW8(m, o, v, r0, r1, r2, r3, r4, r5, r6, r7, k)                                                                                 \
  asm volatile("s_mov_b32 m0, %0\n\ts_nop 0\n\tds_write_addtid_b32 %1 offset:%9\n\tds_write_addtid_b32 %2 offset:%10\n\t"                \
               "ds_write_addtid_b32 %3 offset:%11\n\tds_write_addtid_b32 %4 offset:%12\n\tds_write_addtid_b32 %5 offset:%13\n\t"         \
               "ds_write_addtid_b32 %6 offset:%14\n\tds_write_addtid_b32 %7 offset:%15\n\tds_write_addtid_b32 %8 offset:%16" ::"s"(m),   \
               "v"(v[r0]), "v"(v[r1]), "v"(v[r2]), "v"(v[r3]), "v"(v[r4]), "v"(v[r5]), "v"(v[r6]), "v"(v[r7]), "n"((o) + (r0 + k) * TP),  \
               "n"((o) + (r1 + k) * TP), "n"((o) + (r2 + k) * TP), "n"((o) + (r3 + k) * TP), "n"((o) + (r4 + k) * TP),                    \
               "n"((o) + (r5 + k) * TP), "n"((o) + (r6 + k) * TP), "n"((o) + (r7 + k) * TP) : "memory")
typedef v2u v2u_a4 __attribute__((aligned(4)));
__device__ __forceinline__ v2u ld3(const u16 *a, int d, int lane) {
  return *(const GAS v2u_a4 *)((const GAS unsigned char *)a + d * (NM * 2) + ((6 * lane) & ~3));
}
__device__ __forceinline__ void get3(v2u v, int lane, float (&m)[3]) {
  const unsigned sh = (lane & 1) * 16, t = __builtin_amdgcn_alignbit(v.y, v.x, sh);
  m[0] = bflo(t); m[1] = bfhi(t); m[2] = bflo(v.y >> sh);
}
__device__ __forceinline__ int frag5_off(int tq, int j, int g) {
  return ((tq >> 6) * 80 + ((5 * j + (j & 1)) >> 1) * 4 + (tq & 3)) * 1024 + (((tq >> 2) & 15) + 16 * g) * 16;
}
__device__ __forceinline__ v4u widen(v2u v) {
  auto a = __builtin_amdgcn_permlane16_swap(v.x, v.x, false, false);
  auto b = __builtin_amdgcn_permlane16_swap(v.y, v.y, false, false);
  const unsigned a0 = a[0], a1 = a[1], b0 = b[0], b1 = b[1];
  return v4u{a0, b0, a1, b1};
}
#define PIN(x) asm volatile("" : "+v"(x))
#define UNDEF(x) asm volatile("" : "=v"(x))
#define KEEP8(a) asm volatile("" ::"v"(a[0]), "v"(a[1]), "v"(a[2]), "v"(a[3]), "v"(a[4]), "v"(a[5]), "v"(a[6]), "v"(a[7]))
#define KOFFS(n, base, rot) int ko[n]; _Pragma("unroll") for (int s_ = 0; s_ < n; ++s_) { ko[s_] = (base) + rotk(s_, rot, n) * 4096; asm volatile("" : "+s"(ko[s_])); }
constexpr int issued(int s, int n, int pre, int win) {
  const int a = pre + 2 * s, b = s + win - 1 > pre ? s + win - 1 : pre;
  return (a < b ? a : b) < n ? (a < b ? a : b) : n;
}
__device__ __forceinline__ int rotk(int s, int rot, int n) { const int k = s + rot; return k >= n ? k - n : k; }
__device__ __forceinline__ f32x4 unpack4(v2u v) { return f32x4{bflo(v.x), bfhi(v.x), bflo(v.y), bfhi(v.y)}; }

__device__ __forceinline__ void wait_done(rsrc_t rs, int off, v2u first, u64 want) {
  const v2u t = split64(want);
  if (!__all(first.x == t.x && first.y == t.y)) {
#pragma unroll 1
    for (unsigned spins = 0; spins < SPIN_CAP; ++spins) {
      const v2u v = ld8(rs, off, 0);
      if (__all(v.x == t.x && v.y == t.y)) break;
      __builtin_amdgcn_s_sleep(8);
    }
  }
  asm volatile("" ::: "memory");
}
__device__ __forceinline__ void retile_load(const u16 *w1, const u16 *w2, const u16 *w3, const u16 *w4, int unit, int lane, v4u (&t)[8],
                                            const GAS unsigned char *rows = nullptr) {
  const bool is3 = unit >= 200 && unit < 440;
  const int u = unit < 100 ? unit : unit < 200 ? unit - 100 : is3 ? unit - 200 : unit - 440;
  const int ks = is3 ? (u * 5462) >> 16 : (u * 13108) >> 16, cb = u - ks * (is3 ? 12 : 5), pitch = is3 ? F * 2 : D * 2;
  const GAS unsigned char *src = (const GAS unsigned char *)(unit < 100 ? w1 : unit < 200 ? w2 : is3 ? w3 : w4) + ks * 32 * pitch + cb * 256;
  const unsigned vo = rows ? (lane >> 3) * (D * 2) + (lane & 7) * 16 : (lane >> 4) * pitch + (lane & 15) * 16;
  const int step = rows ? 128 : 4 * pitch;
  if (rows) src = rows;
#pragma unroll
  for (int i = 0; i < 8; ++i) t[i] = *(const GAS v4u *)(src + i * step + vo);
}
__device__ __forceinline__ void retile_store(LAS unsigned char *st, rsrc_t rs, int unit, int lane, v4u (&t)[8]) {
  const bool is3 = unit >= 200 && unit < 440;
  const int u = unit < 100 ? unit : unit < 200 ? unit - 100 : is3 ? unit - 200 : unit - 440;
  const int ks = is3 ? (u * 5462) >> 16 : (u * 13108) >> 16, cb = u - ks * (is3 ? 12 : 5);
  const int dst = unit < 100 ? OFF_WT1 : unit < 200 ? OFF_WT2 : is3 ? OFF_WT3 : OFF_WT4, per = is3 ? 6 : 5, KS = unit < 440 ? 20 : 48;
  const bool nat = unit >= 100 && unit < 440 && ks - ((ks * 13108) >> 16) * 5 == 2;
  const int nt = cb * 8 + (lane & 7), j = (nt * (is3 ? 10923 : 13108)) >> 16, rem = nt - j * per;
  const int place = dst + ((j * KS + ks) * per + ((!is3 && (j & 1)) ? (rem == 0 ? 4 : rem - 1) : rem)) * 1024;
  int so[8];
#pragma unroll
  for (int i = 0; i < 8; ++i) { so[i] = __builtin_amdgcn_readlane(place, i) + lane * 16; PIN(so[i]); }
  LAS unsigned char *w0 = st + ((lane & 15) >> 1) * FP + (lane >> 4) * 32 + (lane & 1) * 16, *w1 = w0 + (nat ? 512 : 128);
  const int step = nat ? 128 : 256;
  PIN(w0); PIN(w1);
  SCHED();
#pragma unroll
  for (int i = 0; i < 8; ++i) *(LAS v4u *)(((i & 1) ? w1 : w0) + (i >> 1) * step) = t[i];
#pragma unroll
  for (int i = 0; i < 8; ++i) t[i] = wfrag(st + i * FP + lane * 8);
#pragma unroll
  for (int i = 0; i < 8; ++i) st16(rs, so[i], 0, t[i]);
}
struct Early { v2u l[6], ep; unsigned ldt; };

__device__ __forceinline__ void role_compute(const Params &p, LAS unsigned char *lds, rsrc_t rs, int w, int lane, const Early &e) {
  u64 tag;
  const int bid = BID, xg = bid & 7, idx = bid >> 3;
  const bool conv = idx < 20;
  const int r16 = lane & 15, g = lane >> 4;
  if (__builtin_expect(conv, 1)) {
    const int c4 = xg * 20 + idx;
    LAS unsigned char *kc = lds + CV_KC + w * CV_KCW;
    LAS unsigned char *cmp = lds + CV_CMP + w * CV_CMPW + 16;
    f32x4 pm0, pm1, pm2;
    if (lane == 0) *(LAS v2u *)(cmp - 8) = v2u{0u, 0u};
    LAS unsigned char *rf = lds + CV_CMP + w * CV_CMPW + CV_RF;
    if (lane < 32) *(LAS unsigned *)(rf + (lane >> 4) * (CV_RFP - 4) + 256 + (lane & 15) * 4) = 0u;
    LAS u16 *x2 = (LAS u16 *)cmp + (510 - 2 * lane), *x2m = x2 - (lane == 63 ? 2 : 1);
    LAS u16 *x3[9];
    {
      const int q = 63 - lane, hb = lane >> 5, base = hb ? 8 * q : 128 + 4 * q;
      constexpr int offA[8] = {4, 3, 2, 2, 2, 1, 0, 0}, offB[8] = {7, 6, 5, 4, 3, 2, 1, 0};
#pragma unroll
      for (int e = 0; e < 8; ++e) x3[e] = (LAS u16 *)cmp + (base + offA[e] + hb * (offB[e] - offA[e]));
      x3[8] = (LAS u16 *)cmp + (base - (lane >= 31 ? 3 : 2));
#pragma unroll
      for (int e = 0; e < 9; ++e) PIN(x3[e]);
      PIN(x2); PIN(x2m);
    }
    LAS u16 *rw = (LAS u16 *)rf + (80 - 64 * g - r16), *rw0 = lane == 0 ? (LAS u16 *)cmp + 512 : rw + 48, *rw1 = lane == 0 ? rw - 100 : rw + 48;
    LAS unsigned char *cw1 = cmp + (g < 2 ? 256 + 64 * g + 4 * r16 : 384 + 64 * (g - 2) + 2 * r16) * 2, *cw2 = cw1 + (g < 2 ? 4 : 64);
    LAS unsigned char *ez = kc + 16 + g * CV_KP + 4096 + min(r16, 5) * 4, *eo = kc + 16 + lane * 16, *ebd = kc + 16 + g * CV_KP + 1984 + r16 * 4;
    unsigned bsel;
    {
      const int lag = 1055 - 2 * r16 - g;
      bsel = (lag >= 1029 ? 0x0c0cu : 0x0100u) | (lag - 1 >= 1029 ? 0x0c0cu : lag - 1 >= 1022 ? 0x0100u : 0x0302u) << 16;
    }
    PIN(rw); PIN(rw0); PIN(rw1); PIN(cw1); PIN(cw2); PIN(ez); PIN(eo); PIN(ebd); PIN(bsel);
    LAS unsigned char *gb[4], *eb, *qb;
#pragma unroll
    for (int k = 0; k < 4; ++k) {
      const int th = 2 * r16 - 4 * (k + 4) - 2 + g;
      gb[k] = lds + ((CV_UE + 4096 + th * 128 + ((th << 3) & 0x70)) ^ (w * 16));
      PIN(gb[k]);
    }
    {
      const int tq_ = 2 * r16 - 3 + g;
      eb = lds + ((lds_addr(gb[0]) + 2048) ^ 64);
      qb = lds + ((CV_UE + 4096 + tq_ * 128 + ((tq_ << 3) & 0x70)) ^ (w * 16));
    }
    LAS unsigned char *f1 = kc + 16 + ((3 - r16) & 3) * CV_KP + (4056 - 2048) + 16 * g - 8 * (r16 >> 2);
    LAS unsigned char *tf = rf + ((r16 & 1) ? 0 : CV_RFP - 4) + (126 - 96 - 2 * r16 + 8 * g) * 2;
    v4u fid, b9, b7;
    {
      const unsigned sh = 8 * g + 15 - r16;
      const unsigned mi = (unsigned)((1ull << 31) >> sh) & 0xffu, m9 = (unsigned)((0x1ff0ull << 15) >> sh) & 0xffu, m7 = (unsigned)((0xfe0ull << 15) >> sh) & 0xffu;
      const unsigned si = mi | mi << 15, s9 = m9 | m9 << 15, s7 = m7 | m7 << 15;
#pragma unroll
      for (int k = 0; k < 4; ++k) {
        fid[k] = ((si >> 2 * k) & 0x10001u) * 0x3f80u; b9[k] = ((s9 >> 2 * k) & 0x10001u) * 0x3f80u; b7[k] = ((s7 >> 2 * k) & 0x10001u) * 0x3f80u;
      }
    }
    PIN(eb); PIN(qb); PIN(f1); PIN(tf); PIN(fid); PIN(b9); PIN(b7);
    SCHED();
    f32x4 kacc = {0.f, 0.f, 0.f, 0.f}, kac1 = {0.f, 0.f, 0.f, 0.f};
    float kinv;
    v4u ta[4], t1[4], tb[4];
#pragma unroll
    for (int s = 0; s < 4; ++s) { UNDEF(ta[s]); UNDEF(t1[s]); UNDEF(tb[s]); }
    int oc = w * KT_SH, on = oc + 4 * KT_SH;
    const int mp = KT_PV - KT_PO + w * KT_PW;
    LAS unsigned char *trd = lds + r16 * TP + g * 16;
    SCHED();
    {
      const float dt = bflo(pk2(__expf((w & 1) ? bfhi(e.ldt) : bflo(e.ldt)), 0.f));
      float ar[3], ai[3], br[3], bi[3], cr[3], ci[3];
      get3(e.l[0], lane, ar); get3(e.l[1], lane, ai); get3(e.l[2], lane, br); get3(e.l[3], lane, bi); get3(e.l[4], lane, cr); get3(e.l[5], lane, ci);
      f32x4 pm[3];
#pragma unroll
      for (int k = 0; k < 3; ++k) {
        const f32x2 cb = cmul(f32x2{cr[k], ci[k]}, f32x2{dt * br[k], dt * bi[k]});
        pm[k] = f32x4{dt * ar[k] * 1.4426950408889634f, dt * ai[k] * 0.15915494309189535f, cb.x, -cb.y};
      }
      float mx = fmaxf(fmaxf(fmaxf(fabsf(pm[0].z), fabsf(pm[0].w)), fmaxf(fabsf(pm[1].z), fabsf(pm[1].w))), fmaxf(fabsf(pm[2].z), fabsf(pm[2].w)));
      mx = dpp_max<0x121>(dpp_max<0x122>(dpp_max<0x124>(dpp_max<0x128>(mx))));
      const unsigned m0 = __builtin_bit_cast(unsigned, mx);
      const unsigned eb = max(max((unsigned)__builtin_amdgcn_readlane(m0, 0), (unsigned)__builtin_amdgcn_readlane(m0, 16)),
                              max((unsigned)__builtin_amdgcn_readlane(m0, 32), (unsigned)__builtin_amdgcn_readlane(m0, 48))) & 0x7f800000u;
      const float sc = __builtin_bit_cast(float, 0x7f000000u - min(eb, 0x7e800000u));
      kinv = __builtin_bit_cast(float, min(eb, 0x7e800000u));
      if (lane == 0) *(LAS float *)(lds + L_TAG + 16 + w * 4) = kinv;
#pragma unroll
      for (int k = 0; k < 3; ++k) { pm[k].z *= sc; pm[k].w *= sc; }
      pm0 = pm[0]; pm1 = pm[1]; pm2 = pm[2];
    }
#pragma unroll 1
    for (int r = 0; r < 3; ++r) {
      const float ar2 = pm0.x, af = pm0.y;
      const f32x2 z0 = {pm0.z, pm0.w};
      pm0 = pm1; pm1 = pm2;
      const f32x2 e8 = cexp_l(ar2, -af, 8.f), e128 = cexp_l(ar2, af, 128.f), h0 = cexp_l(ar2, af, 1024.f);
      SCHED();
      unsigned vo[16], vh[9];
      f32x2 a2[2], b2[2], c1[1];
      cmulN<2>(a2, {e8, e128}, {e8, e128});
      vo[0] = ph(z0); vh[0] = ph(h0);
      const unsigned g8 = ph(e8), g128 = ph(e128);
      cmulN<2>(b2, {a2[0], a2[1]}, {a2[0], a2[1]});
      const unsigned g16 = ph(a2[0]), g256 = ph(a2[1]);
      cmulN<1>(c1, {b2[0]}, {b2[0]});
      const unsigned g32 = ph(b2[0]), g512 = ph(b2[1]), g64 = ph(c1[0]);
      if (r > 0) {
#pragma unroll
        for (int s = 0; s < 4; ++s) { kacc = mfma_h(ta[s], tb[s], kacc); kac1 = mfma_h(t1[s], tb[s], kac1); }
      }
      SCHED();
      asm volatile("s_mov_b32 m0, %0\n\ts_nop 0\n\tds_write_addtid_b32 %1 offset:%2" ::"s"(mp), "v"(vh[0]), "n"(KT_PO + 16 * TP) : "memory");
      unsigned d3[3], d4[4], d8[8], f8[8];
      hmulN<3>(d3, {vo[0], vh[0], vh[0]}, {g8, g128, vh[0]});
      vo[1] = d3[0]; vh[1] = d3[1]; vh[8] = d3[2];
      hmulN<4>(d4, {vo[0], vo[1], vh[0], vh[1]}, {g16, g16, g256, g256});
      vo[2] = d4[0]; vo[3] = d4[1]; vh[2] = d4[2]; vh[3] = d4[3];
      TW4(oc, 0, 0, 1, 2, 3, vo[0], vo[1], vo[2], vo[3]);
      hmulN<8>(d8, {vo[0], vo[1], vo[2], vo[3], vh[0], vh[1], vh[2], vh[3]}, {g32, g32, g32, g32, g512, g512, g512, g512});
#pragma unroll
      for (int i = 0; i < 4; ++i) { vo[4 + i] = d8[i]; vh[4 + i] = d8[4 + i]; }
      TW8(mp, KT_PO, vh, 1, 2, 3, 4, 5, 6, 7, 8, 16);
      TW4(oc, 0, 4, 5, 6, 7, vo[4], vo[5], vo[6], vo[7]);
      hmulN<8>(f8, {vo[0], vo[1], vo[2], vo[3], vo[4], vo[5], vo[6], vo[7]}, {g64, g64, g64, g64, g64, g64, g64, g64});
#pragma unroll
      for (int i = 0; i < 8; ++i) vo[8 + i] = f8[i];
      TW8(oc, 0, vo, 8, 9, 10, 11, 12, 13, 14, 15, 0);
      KEEP8(vo); KEEP8((vo + 8)); KEEP8((vh + 1));
      PIN(kacc); PIN(kac1);
      BARRIER();

#pragma unroll
      for (int s = 0; s < 4; ++s) {
        tb[s] = *(LAS v4u *)(trd + oc + s * 64);
        ta[s] = *(LAS v4u *)(trd + mp + KT_PO + 16 * TP + s * 64);
        t1[s] = *(LAS v4u *)(trd + oc + 16 * TP + s * 64);
      }
      const int t_ = oc; oc = on; on = t_;
      SCHED();
    }
    SCHED();
#pragma unroll
    for (int s = 0; s < 4; ++s) { kacc = mfma_h(ta[s], tb[s], kacc); kac1 = mfma_h(t1[s], tb[s], kac1); }
    kacc *= kinv; kac1 *= kinv;
    {
      const unsigned a = pk2(kacc.x, kacc.y), b = pk2(kacc.z, kacc.w);
      rw0[0] = (u16)a; rw[32] = (u16)(a >> 16); rw[16] = (u16)b; rw[0] = (u16)(b >> 16);
      rw1[CV_RFP / 2 - 2] = (u16)a; rw[CV_RFP / 2 + 30] = (u16)(a >> 16); rw[CV_RFP / 2 + 14] = (u16)b; rw[CV_RFP / 2 - 2] = (u16)(b >> 16);
      *(LAS unsigned *)(cw1) = pk2(kac1.x, kac1.y);
      *(LAS unsigned *)(cw2) = pk2(kac1.z, kac1.w);
    }
    BARRIER();
    {
      const unsigned V0 = x2[0], V1 = x2[1], V2 = x2[2], Va = x2m[0], V9 = x2m[1];
      unsigned v[11];
#pragma unroll
      for (int e = 0; e < 8; ++e) v[e] = x3[e][0];
      v[10] = x3[8][0]; v[9] = x3[8][1]; v[8] = x3[8][2];
      *(LAS unsigned *)(ez) = 0u;
      *(LAS v4u *)(lds + CV_UE + (w * 64 + lane) * 16) = v4u{0u, 0u, 0u, 0u};
      SCHED();
#define PK(a, b) __builtin_amdgcn_perm(b, a, 0x05040100u)
      v4u qd[8];
#define PUT4(k, w0, w1, w2, w3, w4, a0, a1, a2, a3, a4)                                                                    \
  qd[4 * k] = v4u{w0, w1, w2, w3}; qd[4 * k + 1] = v4u{a0, a1, a2, a3}; qd[4 * k + 2] = v4u{w1, w2, w3, w4}; qd[4 * k + 3] = v4u{a1, a2, a3, a4}; \
  _Pragma("unroll") for (int c_ = 0; c_ < 4; ++c_) { PIN(qd[4 * k + c_]); *(LAS v4u *)(eo + (k + 2) * 1024 + c_ * CV_KP) = qd[4 * k + c_]; }
      PUT4(0, PK(V2, V2), PK(V1, V1), PK(V1, V0), PK(V0, V0), PK(V0, V9), PK(V2, V1), PK(V1, V1), PK(V0, V0), PK(V0, V0), PK(V9, Va))
      PUT4(1, PK(v[0], v[1]), PK(v[2], v[3]), PK(v[4], v[5]), PK(v[6], v[7]), PK(v[8], v[9]), PK(v[1], v[2]), PK(v[3], v[4]), PK(v[5], v[6]),
           PK(v[7], v[8]), PK(v[9], v[10]))
      *(LAS unsigned *)(ebd) = __builtin_amdgcn_perm(0u, (unsigned)__builtin_amdgcn_readfirstlane(qd[1].x), bsel);
      KEEP8(qd);
#undef PUT4
#undef PK
    }
    SCHED();
    v4u Gf[2][8], Ff[2][4], Tf[4], F4;
#define CV_LOADF(rho, buf) _Pragma("unroll") for (int i = 0; i < 4; ++i) Ff[buf][i] = *(LAS v4u_a8 *)(f1 + 64 + (7 - (rho)) * 64 + (3 - i) * 512);
#define CV_LOADG(rho, buf) _Pragma("unroll") for (int i = 0; i < 8; ++i) Gf[buf][i] = *(LAS v4u *)(gb[(rho) & 3] + ((rho) < 4 ? 2048 : 0) + i * 4096);
    CV_LOADF(0, 0)
    F4 = *(LAS v4u_a8 *)(f1);
#pragma unroll
    for (int i = 0; i < 4; ++i) Tf[i] = *(LAS v4u_a8 *)(tf + (3 - i) * 64);
    tag = uni64(*(LAS v2u *)(lds + L_TAG));
    BARRIER();
    f32x4 acc[8];
    v4u Qf[4];
    LAS unsigned char *xw, *xr;
    int ho, ho2;
    {
      v4u Ge[4];
#pragma unroll
      for (int J = 0; J < 4; ++J) Ge[J] = *(LAS v4u *)(qb + J * 4096);
      CV_LOADG(0, 0)
#pragma unroll
      for (int J = 0; J < 8; ++J) Gf[1][J] = *(LAS v4u *)(eb + J * 4096);
      SCHED();
      f32x4 qe[4], qo[4];
#pragma unroll
      for (int J = 0; J < 4; ++J) qe[J] = mfma(Ge[J], b9, f32x4{0.f, 0.f, 0.f, 0.f});
#pragma unroll
      for (int J = 0; J < 4; ++J) qo[J] = mfma(Gf[0][J], b7, f32x4{0.f, 0.f, 0.f, 0.f});
#pragma unroll
      for (int J = 0; J < 8; ++J) acc[J] = mfma(fid, Gf[1][J], f32x4{0.f, 0.f, 0.f, 0.f});
#pragma unroll
      for (int J = 0; J < 4; ++J) acc[4 + J] = mfma(F4, Gf[0][J], acc[4 + J]);
#pragma unroll
      for (int J = 0; J < 4; ++J) Qf[J] = v4u{pk2(qe[J].x, qo[J].x), pk2(qe[J].y, qo[J].y), pk2(qe[J].z, qo[J].z), pk2(qe[J].w, qo[J].w)};
      SGB(0x8, 12);
#pragma unroll
      for (int k = 0; k < 8; ++k) { SGB(0x8, 1); SGB(0x2, 2); }
      SCHED();
    }
#pragma unroll
    for (int rho = 0; rho < 7; ++rho) {
      CV_LOADG(rho + 1, (rho + 1) & 1) CV_LOADF(rho + 1, (rho + 1) & 1)
#pragma unroll
      for (int Dd = 0; Dd < 4; ++Dd)
#pragma unroll
        for (int Jp = 0; Jp + Dd < 8; ++Jp) acc[Dd + Jp] = mfma(Ff[rho & 1][Dd], Gf[rho & 1][Jp], acc[Dd + Jp]);
      if (rho == 0) {
#pragma unroll
        for (int sq = 0; sq < 4; ++sq)
#pragma unroll
          for (int J = sq; J < 4; ++J) acc[4 + J] = mfma(Qf[sq], Tf[J - sq], acc[4 + J]);
      }
      if (rho == 1) {
        xw = lds + CV_X + w * 4096 + lane * 16; xr = lds + CV_X + w * 1024 + lane * 16;
        ho = OFF_H + (((8 * w + (r16 >> 2)) * 160 + c4) * 32 + 4 * (r16 & 3) + g) * 16; ho2 = ho + 4 * 160 * 512;
      }
#pragma unroll
      for (int k = 0; k < 12; ++k) { SGB(0x8, 1); SGB(0x2, 1); SGB(0x8, 1); SGB(0x100, 1); }
      SGB(0x8, 12);
      SCHED();
      if (rho == 1) { PIN(xw); PIN(xr); PIN(ho); PIN(ho2); }
    }
#pragma unroll
    for (int k = 3; k >= 0; --k) {
#pragma unroll
      for (int Dd = 0; Dd < 4; ++Dd)
#pragma unroll
        for (int J = 2 * k; J < 2 * k + 2; ++J)
          if (J >= Dd) acc[J] = mfma(Ff[1][Dd], Gf[1][J - Dd], acc[J]);
      if (k < 3) {
        const int m = k + 1;
        *(LAS v4u *)(xw + m * 1024) = v4u{pk2(acc[2 * m].x, acc[2 * m].y), pk2(acc[2 * m].z, acc[2 * m].w), pk2(acc[2 * m + 1].x, acc[2 * m + 1].y),
                                          pk2(acc[2 * m + 1].z, acc[2 * m + 1].w)};
        SGB(0x8, 2); SGB(0x2, 2); SGB(0x8, 1); SGB(0x2, 2); SGB(0x200, 1); SGB(0x8, 5);
      }
      SCHED();
    }
#undef CV_LOADF
#undef CV_LOADG
    *(LAS v4u *)(xw) = v4u{pk2(acc[0].x, acc[0].y), pk2(acc[0].z, acc[0].w), pk2(acc[1].x, acc[1].y), pk2(acc[1].z, acc[1].w)};
    BARRIER();
    v4u v[4];
#pragma unroll
    for (int c = 0; c < 4; ++c) v[c] = *(LAS v4u *)(xr + c * 4096);
    const unsigned sa = xg < 4 ? 0x05040100u : 0x07060302u, sb = xg < 4 ? 0x07060302u : 0x05040100u;
#pragma unroll
    for (int z = 0; z < 4; ++z) {
      const v4u ch = {__builtin_amdgcn_perm(v[1][z], v[0][z], sa), __builtin_amdgcn_perm(v[3][z], v[2][z], sa),
                      __builtin_amdgcn_perm(v[1][z], v[0][z], sb), __builtin_amdgcn_perm(v[3][z], v[2][z], sb)};
      st16(rs, (z >> 1) ? ho2 : ho, (z & 1) * 256, ch);
    }
    BARRIER();
    arrive(p.ws, C_S1 + ((8 * w + (lane & 7)) * 8 + xg) * 64, lane < 8);
  } else {
    BARRIER();
    tag = uni64(*(LAS v2u *)(lds + L_TAG));
    BARRIER();
    BARRIER();
  }
  const u64 cbase = tag << 8;
  const int rt = 4 * xg + (idx >> 3), j = idx & 7, ps = w & 1, kh = w >> 1, q = 2 * ps + kh;
  const int tq = rt * 64 + 4 * r16 + q;
  const int rot10 = (j * 5) >> 2, rot2 = (j & 1) * 5;
  const int single = ((j & 1) ? -4096 : 8192) + (((g >> 1) + 2 * (j & 1)) - g) * 256;
  LAS unsigned char *xo = lds + G_X + w * 5120 + lane * 16, *xi = lds + G_X + (w ^ 2) * 5120 + lane * 16;
  LAS unsigned char *cr = lds + G_CS + g * 16;
  PIN(cr);
  float mean1, rstd1;
  unsigned pf0 = 0, pf1 = 0;
  int a_off, p_off0, p_off1;
  bool a_on;
#define ARRIVE() arrive(p.ws, a_off, a_on)
#define WAIT1(dly, off, want) wait_cnt_pf<dly>(p.ws, off, want, p_off0, p_off1, pf0, pf1)
  v2u hv[5];
  f32x4 res[5];
  const int hoff = (g * 32 + (q >> 1) * 16 + r16) * 16 + ((q ^ (j >> 2)) & 1) * 8;
  {
    const int av = (g * 32 + r16) * 16, as = OFF_H + ((rt * 160 + kh * 80) * 32 + ps * 16) * 16;
    const int qd = q + shift_of(j), carry = qd >> 2;
    int so = OFF_H1 + frag5_off(tq, j, g), so1 = so + 4096, so2 = so + single;
    int co = C_S2 + ((((rt + (lane ? carry : 0)) & 31) * 2 + ((qd >> 1) & 1)) * 2 + (j >> 2)) * 64;
    LAS unsigned char *sw = lds + G_S + ((q * 16 + r16) * 2 + kh) * 8, *sr = lds + G_S + (q * 16 + r16) * 16;
    PIN(so); PIN(so1); PIN(so2); PIN(co); PIN(sw); PIN(sr); PIN(xo); PIN(xi);
    KOFFS(10, as, rot10)
    wait_cnt<1>(p.ws, C_S1 + (rt * 8 + kh * 4 + (lane & 3)) * 64, lane < 4, cbase + 20);
    v4u la[10], lb[10];
#pragma unroll
    for (int s = 0; s < G12_PRE; ++s) { la[s] = ld16(rs, av, ko[s]); lb[s] = ld16(rs, av + 2048, ko[s]); SCHED(); }
    BARRIER();
    LAS unsigned char *wb = lds + kh * 50 * 1024 + lane * 16;
    PIN(wb);
    constexpr int WD = 4;
    v4u wf[WD][5];
#pragma unroll
    for (int s = 0; s < WD - 1; ++s)
#pragma unroll
      for (int i = 0; i < 5; ++i) wf[s][i] = *(LAS v4u *)(wb + (s * 5 + i) * 1024);
    f32x4 acc[2][5], sm[2], gr[2];
#pragma unroll
    for (int i = 0; i < 5; ++i) { acc[0][i] = f32x4{0.f, 0.f, 0.f, 0.f}; acc[1][i] = f32x4{0.f, 0.f, 0.f, 0.f}; }
    sm[0] = sm[1] = gr[0] = gr[1] = f32x4{0.f, 0.f, 0.f, 0.f};
    v4u ones = {ONE2, ONE2, ONE2, ONE2};
    PIN(ones);
    SCHED();
#pragma unroll
    for (int s = 0; s < 10; ++s) {
      if (s == 2) BARRIER();
      v4u fa = la[s], fb = lb[s];
      {
        unsigned t0 = fa.z, t1 = fa.w, t2 = fb.x, t3 = fb.y;
        asm("v_swap_b32 %0, %1\n\tv_swap_b32 %2, %3" : "+v"(t0), "+v"(t2), "+v"(t1), "+v"(t3));
        fa.z = t0; fa.w = t1; fb.x = t2; fb.y = t3;
        asm volatile("s_nop 1" : "+v"(fa), "+v"(fb));
      }
      if (s > 0 && s < 9) {
        if (s < 8) WAIT_LGKM(10); else WAIT_LGKM(5);
        SCHED();
#pragma unroll
        for (int m = 0; m < 14; ++m) {
          if (m == 0) MF(sm[0], ones, fa); else if (m == 1) MF(gr[0], fa, fa); else if (m == 2) MF(sm[1], ones, fb); else if (m == 3) MF(gr[1], fb, fb);
          else if (m < 9) MF(acc[1][(m + 1) % 5], wf[s % WD][(m + 1) % 5], fb); else MF(acc[0][(m + 1) % 5], wf[s % WD][(m + 1) % 5], fa);
          const int k = G12_PRE + 2 * s + m / 2;
          if (m < 4) {
            if (k < 10) { if (m & 1) lb[k < 10 ? k : 0] = ld16(rs, av + 2048, ko[k < 10 ? k : 0]); else la[k < 10 ? k : 0] = ld16(rs, av, ko[k < 10 ? k : 0]); }
          } else if (!(m & 1) && s + WD - 1 < 10) {
            wf[(s + WD - 1) % WD][(m - 4) / 2] = *(LAS v4u *)(wb + ((s + WD - 1) * 5 + (m - 4) / 2) * 1024);
          }
          SCHED();
        }
        continue;
      }
#pragma unroll
      for (int k = G12_PRE + 2 * s; k < G12_PRE + 2 * s + 2 && k < 10; ++k) {
        la[k] = ld16(rs, av, ko[k]); lb[k] = ld16(rs, av + 2048, ko[k]);
      }
      if (s + WD - 1 < 10) {
#pragma unroll
        for (int i = 0; i < 5; ++i) wf[(s + WD - 1) % WD][i] = *(LAS v4u *)(wb + ((s + WD - 1) * 5 + i) * 1024);
      }
      sm[0] = mfma(ones, fa, sm[0]); gr[0] = mfma(fa, fa, gr[0]);
      sm[1] = mfma(ones, fb, sm[1]); gr[1] = mfma(fb, fb, gr[1]);
#pragma unroll
      for (int i = 0; i < 5; ++i) acc[1][i] = mfma(wf[s % WD][i], fb, acc[1][i]);
#pragma unroll
      for (int i = 0; i < 5; ++i) acc[0][i] = mfma(wf[s % WD][i], fa, acc[0][i]);
      if (s == 9) {
#pragma unroll
        for (int i = 0; i < 5; ++i) *(LAS f32x4 *)(xo + i * 1024) = acc[1][i];
        SGB(0x8, 11);
#pragma unroll
        for (int k = 0; k < 3; ++k) { SGB(0x200, 1); SGB(0x8, 1); }
        SGB(0x200, 2);
      } else {
#pragma unroll
      for (int k = 0; k < 4; ++k) { SGB(0x8, 1); SGB(0x20, 1); }
#pragma unroll
      for (int k = 0; k < 5; ++k) { SGB(0x8, 2); SGB(0x100, 1); }
      }
      SCHED();
    }
    if (g == (r16 >> 2)) {
      const int e = r16 & 3;
      *(LAS f32x2 *)(sw) = f32x2{sm[0].x, e == 0 ? gr[0].x : e == 1 ? gr[0].y : e == 2 ? gr[0].z : gr[0].w};
      *(LAS f32x2 *)(sw + (kh ? -256 : 256)) = f32x2{sm[1].x, e == 0 ? gr[1].x : e == 1 ? gr[1].y : e == 2 ? gr[1].z : gr[1].w};
    }
    BARRIER();
#pragma unroll
    for (int i = 0; i < 5; ++i) hv[i] = ld8(rs, hoff, OFF_H + (rt * 160 + ntile5(j, i) * 4) * 512);
    const f32x4 st = *(LAS f32x4 *)(sr);
    f32x4 v[5], cs[5];
#pragma unroll
    for (int i = 0; i < 5; ++i) { v[i] = *(LAS f32x4 *)(xi + i * 1024); cs[i] = *(LAS f32x4 *)(cr + i * 64); }
    SCHED();
    mean1 = (st.x + st.z) * (1.f / D);
    const float var = fmaxf((st.y + st.w) * (1.f / D) - mean1 * mean1, 0.f);
    rstd1 = __builtin_amdgcn_rsqf(var + EPS);
    const float r2 = __builtin_amdgcn_rsqf(var * rstd1 * rstd1 + EPS);
    const float rr = rstd1 * r2, nm = -mean1 * rr;
    v2u pk[5];
#pragma unroll
    for (int i = 0; i < 5; ++i) {
      const f32x4 pre = (acc[0][i] + v[i]) * rr + cs[i] * nm;
      pk[i] = v2u{pk2(gelu2(f32x2{pre.x, pre.y})), pk2(gelu2(f32x2{pre.z, pre.w}))};
      if (i == 1) st16(rs, so, 0, v4u{pk[0].x, pk[0].y, pk[1].x, pk[1].y});
      if (i == 3) st16(rs, so1, 0, v4u{pk[2].x, pk[2].y, pk[3].x, pk[3].y});
    }
    { const v4u w4 = widen(pk[4]); if (!(g & 1)) st16(rs, so2, 0, w4); }
    BARRIER();
    a_off = co; a_on = lane == 0 || (lane == 1 && carry != 0); p_off0 = (lane & 1) ? so1 : so; p_off1 = so2;
    if (bid == 0 && w == 0 && lane == 0) st8(rs, 0, 0, split64(tag));
  }
  const int ava = q * 1024 + lane * 16, avb = (q ^ 1) * 1024 + lane * 16;
  {
    int sva[2], svb[2];
#pragma unroll
    for (int h = 0; h < 2; ++h) {
      const int sh = shift_of(4 * kh + 2 * (h ^ (rot2 ? 1 : 0)));
      const int ta_ = tq - sh, tb_ = (tq ^ 1) - sh;
      sva[h] = (unsigned)ta_ < (unsigned)L ? ((ta_ >> 6) * 80 + (ta_ & 3)) * 1024 + (((ta_ >> 2) & 15) + 16 * g) * 16 : 0x40000000;
      svb[h] = (unsigned)tb_ < (unsigned)L ? ((tb_ >> 6) * 80 + (tb_ & 3)) * 1024 + (((tb_ >> 2) & 15) + 16 * g) * 16 : 0x40000000;
      PIN(sva[h]); PIN(svb[h]);
    }
    f32x4 acc[2][5];
    const float nm = -mean1 * rstd1;
#pragma unroll
    for (int i = 0; i < 5; ++i) { acc[0][i] = unpack4(hv[i]) * rstd1 + nm; PIN(acc[0][i]); acc[1][i] = f32x4{0.f, 0.f, 0.f, 0.f}; }
    ARRIVE();
    int so = OFF_X1 + frag5_off(tq, j, g), so1 = so + 4096, so2 = so + single, so3 = OFF_XS + (((rt * 4 + q) * 8 + j) * 16 + r16) * 8;
    PIN(so); PIN(so1); PIN(so2); PIN(so3);
    KOFFS(10, OFF_H1 + kh * 10 * 4096, rot2)
    WAIT1(3, C_S2 + ((rt * 2 + ps) * 2 + kh) * 64, cbase + 10 + 2 * kh);
    v4u la[10], lb[10];
#pragma unroll
    for (int s = 0; s < G12_PRE; ++s) { lb[s] = ld16(rs, svb[s / 5], ko[s]); la[s] = ld16(rs, sva[s / 5], ko[s]); SCHED(); }
    BARRIER();
    LAS unsigned char *wb = lds + kh * 50 * 1024 + lane * 16;
    PIN(wb);
    constexpr int WD = 5;
    v4u wf[WD][5];
#pragma unroll
    for (int s = 0; s < WD - 1; ++s)
#pragma unroll
      for (int i = 0; i < 5; ++i) wf[s][i] = *(LAS v4u *)(wb + (s * 5 + i) * 1024);
    SCHED();
    SCHED();
#pragma unroll
    for (int s = 0; s < 10; ++s) {
      if (s == 2) BARRIER();
      if (s > 0 && s < 9) {
        if (s < 7) WAIT_LGKM(15); else if (s == 7) WAIT_LGKM(10); else WAIT_LGKM(5);
        SCHED();
#pragma unroll
        for (int m = 0; m < 10; ++m) {
          if (m < 5) MF(acc[1][m % 5], wf[s % WD][m % 5], lb[s]); else MF(acc[0][m % 5], wf[s % WD][m % 5], la[s]);
          const int k = G12_PRE + 2 * s + m / 4;
          if (!(m & 1)) {
            if (s + WD - 1 < 10) wf[(s + WD - 1) % WD][m / 2] = *(LAS v4u *)(wb + ((s + WD - 1) * 5 + m / 2) * 1024);
          } else if (m < 8 && k < 10) {
            if (m & 2) la[k < 10 ? k : 0] = ld16(rs, sva[k < 10 ? k / 5 : 0], ko[k < 10 ? k : 0]); else lb[k < 10 ? k : 0] = ld16(rs, svb[k < 10 ? k / 5 : 0], ko[k < 10 ? k : 0]);
          }
          SCHED();
        }
        continue;
      }
#pragma unroll
      for (int k = G12_PRE + 2 * s; k < G12_PRE + 2 * s + 2 && k < 10; ++k) {
        lb[k] = ld16(rs, svb[k / 5], ko[k]); la[k] = ld16(rs, sva[k / 5], ko[k]);
      }
      if (s + WD - 1 < 10) {
#pragma unroll
        for (int i = 0; i < 5; ++i) wf[(s + WD - 1) % WD][i] = *(LAS v4u *)(wb + ((s + WD - 1) * 5 + i) * 1024);
      }
#pragma unroll
      for (int i = 0; i < 5; ++i) acc[1][i] = mfma(wf[s % WD][i], lb[s], acc[1][i]);
#pragma unroll
      for (int i = 0; i < 5; ++i) acc[0][i] = mfma(wf[s % WD][i], la[s], acc[0][i]);
      if (s == 9) {
#pragma unroll
        for (int i = 0; i < 5; ++i) *(LAS f32x4 *)(xo + i * 1024) = acc[1][i];
        SGB(0x8, 7);
#pragma unroll
        for (int k = 0; k < 3; ++k) { SGB(0x200, 1); SGB(0x8, 1); }
        SGB(0x200, 2);
      } else {
#pragma unroll
      for (int k = 0; k < 5; ++k) { SGB(0x8, 1); SGB(0x100, 1); SGB(0x8, 1); SGB(0x20, 1); }
      }
      SCHED();
    }
    asm volatile("" ::"v"(pf0), "v"(pf1));
    BARRIER();
    v2u pk[5];
    f32x4 v[5];
#pragma unroll
    for (int i = 0; i < 5; ++i) v[i] = *(LAS f32x4 *)(xi + i * 1024);
    SCHED();
#pragma unroll
    for (int i = 0; i < 5; ++i) {
      const f32x4 x1 = acc[0][i] + v[i];
      pk[i] = v2u{pk2(x1.x, x1.y), pk2(x1.z, x1.w)};
    }
    st16(rs, so, 0, v4u{pk[0].x, pk[0].y, pk[1].x, pk[1].y});
    st16(rs, so1, 0, v4u{pk[2].x, pk[2].y, pk[3].x, pk[3].y});
    { const v4u w4 = widen(pk[4]); if (!(g & 1)) st16(rs, so2, 0, w4); }
    SCHED();
    {
      float s0 = 0.f, q0 = 0.f, s1 = 0.f, q1 = 0.f;
#pragma unroll
      for (int i = 0; i < 5; ++i) {
        s0 = dot2(pk[i].x, ONE2, s0); q0 = dot2(pk[i].x, pk[i].x, q0); s1 = dot2(pk[i].y, ONE2, s1); q1 = dot2(pk[i].y, pk[i].y, q1);
      }
      s0 = sum_x32(sum_x16(s0 + s1)); q0 = sum_x32(sum_x16(q0 + q1));
      if (g == 0) st8(rs, so3, 0, v2u{__builtin_bit_cast(unsigned, s0), __builtin_bit_cast(unsigned, q0)});
    }
    BARRIER();
    a_off = C_S3 + (rt * 2 + ps) * 64; a_on = lane == 0; p_off0 = (lane & 1) ? so1 : so; p_off1 = (g & 1) ? so3 : so2;
#pragma unroll
    for (int i = 0; i < 5; ++i) { res[i] = unpack4(hv[i]) + unpack4(pk[i]); PIN(res[i]); }
  }
  {
    const int j3 = idx & 15, rt3 = 4 * xg + 2 * (idx >> 4) + (w >> 1), q0 = 2 * (w & 1);
    const int rot20 = (j3 & 3) * 5;
    int so = OFF_HID + ((rt3 * 48 + j3 * 3) * 4 + q0) * 1024 + lane * 16;
    PIN(so);
    KOFFS(20, OFF_X1 + (rt3 * 80 + q0) * 1024, rot20)
    ARRIVE();
    WAIT1(6, C_S3 + (rt3 * 2 + (w & 1)) * 64, cbase + 16);
    const int av = lane * 16;
    constexpr int WIN = G3_WIN;
    v4u la[WIN], lb[WIN];
#pragma unroll
    for (int s = 0; s < G3_PRE; ++s) { la[s] = ld16(rs, av, ko[s]); lb[s] = ld16(rs, av + 1024, ko[s]); SCHED(); }
    BARRIER();
    LAS unsigned char *wb = lds + lane * 16, *wb2 = wb + 60 * 1024;
    PIN(wb); PIN(wb2);
    constexpr int WD = 3;
    v4u wf[WD][6];
#pragma unroll
    for (int s = 0; s < WD - 1; ++s)
#pragma unroll
      for (int i = 0; i < 6; ++i) wf[s][i] = *(LAS v4u *)(wb + (s * 6 + i) * 1024);
    static_assert(issued(XS_AT, 20, G3_PRE, WIN) == 20 && XS_AT > 0 && XS_AT < MIDB && MIDB < 16, "");
    v2u xs[2][2];
    float rr[2], nm[2];
    f32x4 cs[6], cn[2][6];
    f32x4 acc[2][6];
#pragma unroll
    for (int i = 0; i < 6; ++i) { acc[0][i] = f32x4{0.f, 0.f, 0.f, 0.f}; acc[1][i] = f32x4{0.f, 0.f, 0.f, 0.f}; }
    SCHED();
#pragma unroll
    for (int s = 0; s < 20; ++s) {

      if (s < 18) WAIT_LGKM(6); else if (s == 18) WAIT_LGKM(12); else WAIT_LGKM(0);
      SCHED();
      if (s == MIDB) BARRIER();
      if (s > 0 && s < 16) {
        const int k0 = issued(s, 20, G3_PRE, WIN), nk = issued(s + 1, 20, G3_PRE, WIN) - k0;
#pragma unroll
        for (int m = 0; m < 12; ++m) {
          if (m < 6) MF(acc[0][m % 6], wf[s % WD][m % 6], la[s % WIN]); else MF(acc[1][m % 6], wf[s % WD][m % 6], lb[s % WIN]);
          if (!(m & 1)) {
            wf[(s + WD - 1) % WD][m / 2] = *(LAS v4u *)(s + WD - 1 < 10 ? wb + ((s + WD - 1) * 6 + m / 2) * 1024 : wb2 + ((s + WD - 11) * 6 + m / 2) * 1024);
          } else if (m / 2 < 2 * nk) {
            const int k = k0 + m / 4;
            if (m & 2) lb[k % WIN] = ld16(rs, av + 1024, ko[k < 20 ? k : 0]); else la[k % WIN] = ld16(rs, av, ko[k < 20 ? k : 0]);
          } else if (s == XS_AT && m / 2 < 4) {
            xs[m / 4][(m / 2) & 1] = ld8(rs, lane * 8 + ((m / 2) & 1) * 512, OFF_XS + (rt3 * 4 + q0 + m / 4) * 1024);
          }
          SCHED();
        }
        continue;
      }
      if (s + WD - 1 < 20) {
#pragma unroll
        for (int i = 0; i < 6; ++i) wf[(s + WD - 1) % WD][i] = *(LAS v4u *)(s + WD - 1 < 10 ? wb + ((s + WD - 1) * 6 + i) * 1024 : wb2 + ((s + WD - 11) * 6 + i) * 1024);
      }
#pragma unroll
      for (int k = issued(s, 20, G3_PRE, WIN); k < issued(s + 1, 20, G3_PRE, WIN); ++k) { la[k % WIN] = ld16(rs, av, ko[k]); lb[k % WIN] = ld16(rs, av + 1024, ko[k]); }
#pragma unroll
      for (int i = 0; i < 6; ++i) acc[0][i] = mfma(wf[s % WD][i], la[s % WIN], acc[0][i]);
#pragma unroll
      for (int i = 0; i < 6; ++i) acc[1][i] = mfma(wf[s % WD][i], lb[s % WIN], acc[1][i]);
      if (s == 16) {
        const f32x2 t0 = __builtin_bit_cast(f32x2, xs[0][0]) + __builtin_bit_cast(f32x2, xs[0][1]), t1 = __builtin_bit_cast(f32x2, xs[1][0]) + __builtin_bit_cast(f32x2, xs[1][1]);
        nm[0] = sum_x32(sum_x16(t0.x)); rr[0] = sum_x32(sum_x16(t0.y)); nm[1] = sum_x32(sum_x16(t1.x)); rr[1] = sum_x32(sum_x16(t1.y));
      }
      if (s == 17) {
#pragma unroll
        for (int m = 0; m < 2; ++m) {
          const float mean = nm[m] * (1.f / D);
          rr[m] = __builtin_amdgcn_rsqf(fmaxf(rr[m] * (1.f / D) - mean * mean, 0.f) + EPS);
          nm[m] = -mean * rr[m];
        }
#pragma unroll
        for (int i = 0; i < 6; ++i) cs[i] = *(LAS f32x4 *)(cr + i * 64);
      }
      if (s >= 18) {
#pragma unroll
        for (int i = 0; i < 6; ++i)
          cn[s - 18][i] = f32x4{mul1(cs[i].x, nm[s - 18]), mul1(cs[i].y, nm[s - 18]),
                                mul1(cs[i].z, nm[s - 18]), mul1(cs[i].w, nm[s - 18])};
      }
      if (s < 16) {
#pragma unroll
        for (int k = 0; k < 6; ++k) { SGB(0x8, 1); SGB(0x100, 1); SGB(0x8, 1); SGB(0x20, 1); }
      } else {
#pragma unroll
        for (int k = 0; k < 12; ++k) { SGB(0x8, 1); SGB(0x100, 1); SGB(0x402, 2); }
      }
      SCHED();
      if (s == 16 || s == 17) { PIN(nm[0]); PIN(nm[1]); PIN(rr[0]); PIN(rr[1]); }
      if (s >= 18) {
#pragma unroll
        for (int i = 0; i < 6; ++i) PIN(cn[s - 18][i]);
      }
    }
    asm volatile("" ::"v"(pf0), "v"(pf1));
#pragma unroll
    for (int m = 0; m < 2; ++m) {
#pragma unroll
      for (int k = 0; k < 3; ++k) {
        const f32x4 pa = acc[m][2 * k] * rr[m] + cn[m][2 * k], pb = acc[m][2 * k + 1] * rr[m] + cn[m][2 * k + 1];
        st16(rs, so, k * 4096 + m * 1024,
             v4u{pk2(gelu2(f32x2{pa.x, pa.y})), pk2(gelu2(f32x2{pa.z, pa.w})), pk2(gelu2(f32x2{pb.x, pb.y})), pk2(gelu2(f32x2{pb.z, pb.w}))});
      }
    }
    BARRIER();
    a_off = C_S4 + ((rt3 * 2 + (w & 1)) * 2 + (j3 >> 3)) * 64; a_on = lane == 0;
    p_off0 = so - lane * 16 + ((lane >> 4) & 1) * 4096 + (lane & 15) * 128;
    p_off1 = so - lane * 16 + 8192 + (lane & 15) * 128;
  }
  {
    const int rot24 = 3 * j;
    LAS unsigned char *xo4 = lds + G4_X + w * 5120 + lane * 16, *xi4 = lds + G4_X + (w ^ 2) * 5120 + lane * 16;
    u16 *orow = p.out + (size_t)tq * D + ntile5(j, 4) * 16 + 4 * g, *orow2 = p.out + (size_t)tq * D + (ntile5(j, 0) + (g & 1)) * 16 + (g >> 1) * 8;
    PIN(xo4); PIN(xi4); PIN(orow); PIN(orow2);
    KOFFS(24, OFF_HID + (rt * 48 + kh * 24) * 4096, rot24)
    ARRIVE();
    WAIT1(6, C_S4 + ((rt * 2 + ps) * 2 + kh) * 64, cbase + 8);
    constexpr int WIN = G4_WIN;
    v4u la[WIN], lb[WIN];
#pragma unroll
    for (int s = 0; s < G4_PRE; ++s) { lb[s] = ld16(rs, avb, ko[s]); la[s] = ld16(rs, ava, ko[s]); SCHED(); }
    BARRIER();
    LAS unsigned char *wb = lds + kh * 70 * 1024 + lane * 16, *wb2 = wb + 50 * 1024;
    PIN(wb); PIN(wb2);
    v4u wf[3][5];
#pragma unroll
    for (int i = 0; i < 10; ++i) wf[i / 5][i % 5] = *(LAS v4u *)(wb + i * 1024);
    SCHED();
    f32x4 acc[2][5];
#pragma unroll
    for (int i = 0; i < 5; ++i) { acc[0][i] = res[i]; acc[1][i] = f32x4{0.f, 0.f, 0.f, 0.f}; }
    SCHED();
#pragma unroll
    for (int s = 0; s < 24; ++s) {
      if (s == 2 || s == 8 || s == 16) BARRIER();
      if (s < 23) WAIT_LGKM(5); else WAIT_LGKM(0);
      SCHED();

      if (s > 0 && s < 23) {
        const int slot = s + 2 < 14 ? s + 2 : s + 2 - 14, k0 = issued(s, 24, G4_PRE, WIN), nk = issued(s + 1, 24, G4_PRE, WIN) - k0;
#pragma unroll
        for (int m = 0; m < 10; ++m) {
          if (m < 5) MF(acc[1][m % 5], wf[s % 3][m % 5], lb[s % WIN]); else MF(acc[0][m % 5], wf[s % 3][m % 5], la[s % WIN]);
          if (!(m & 1)) {
            if (s < 22) wf[(s + 2) % 3][m / 2] = *(LAS v4u *)(slot < 10 ? wb + (slot * 5 + m / 2) * 1024 : wb2 + ((slot - 10) * 5 + m / 2) * 1024);
          } else if (m / 2 < 2 * nk) {
            const int k = k0 + m / 4;
            if (m & 2) la[k % WIN] = ld16(rs, ava, ko[k < 24 ? k : 0]); else lb[k % WIN] = ld16(rs, avb, ko[k < 24 ? k : 0]);
          }
          SCHED();
        }
        continue;
      }
      if (s < 22) {
        const int slot = s + 2 < 14 ? s + 2 : s + 2 - 14;
#pragma unroll
        for (int i = 0; i < 5; ++i) wf[(s + 2) % 3][i] = *(LAS v4u *)(slot < 10 ? wb + (slot * 5 + i) * 1024 : wb2 + ((slot - 10) * 5 + i) * 1024);
      }
#pragma unroll
      for (int k = issued(s, 24, G4_PRE, WIN); k < issued(s + 1, 24, G4_PRE, WIN); ++k) { lb[k % WIN] = ld16(rs, avb, ko[k]); la[k % WIN] = ld16(rs, ava, ko[k]); }
#pragma unroll
      for (int i = 0; i < 5; ++i) acc[1][i] = mfma(wf[s % 3][i], lb[s % WIN], acc[1][i]);
#pragma unroll
      for (int i = 0; i < 5; ++i) acc[0][i] = mfma(wf[s % 3][i], la[s % WIN], acc[0][i]);
      if (s == 23) {
#pragma unroll
        for (int i = 0; i < 5; ++i) *(LAS f32x4 *)(xo4 + i * 1024) = acc[1][i];
        SGB(0x8, 7);
#pragma unroll
        for (int k = 0; k < 3; ++k) { SGB(0x200, 1); SGB(0x8, 1); }
        SGB(0x200, 2);
      } else {
#pragma unroll
      for (int k = 0; k < 5; ++k) { SGB(0x8, 1); SGB(0x100, 1); SGB(0x8, 1); SGB(0x20, 1); }
      }
      SCHED();
    }
    asm volatile("" ::"v"(pf0), "v"(pf1));
    BARRIER();
    f32x4 v[5];
#pragma unroll
    for (int i = 0; i < 5; ++i) v[i] = *(LAS f32x4 *)(xi4 + i * 1024);
    SCHED();
    v2u pk[5];
#pragma unroll
    for (int i = 0; i < 5; ++i) {
      const f32x4 o = acc[0][i] + v[i];
      pk[i] = v2u{pk2(o.x, o.y), pk2(o.z, o.w)};
      if (i & 1) {
        auto a = __builtin_amdgcn_permlane16_swap(pk[i - 1].x, pk[i].x, false, false);
        auto b = __builtin_amdgcn_permlane16_swap(pk[i - 1].y, pk[i].y, false, false);
        const unsigned a0 = a[0], a1 = a[1], b0 = b[0], b1 = b[1];
        *(GAS v4u *)(orow2 + (i - 1) * 16) = v4u{a0, b0, a1, b1};
      }
    }
    *(GAS v2u *)(orow) = pk[4];
  }
}

template <int PER> __device__ __forceinline__ void colsum_part(LAS unsigned char *lds, int x, int lane, const f32x4 (&c)[PER]) {
  if ((lane & 15) == 0) {
#pragma unroll
    for (int i = 0; i < PER; ++i) *(LAS f32x4 *)(lds + G_CS + 512 + x * 384 + i * 64 + (lane >> 4) * 16) = c[i];
  }
}
__device__ __forceinline__ void colsum_add(LAS unsigned char *lds, int x, int lane) {
  if (x < 2 && lane < 48) {
    LAS float *f = (LAS float *)(lds + G_CS) + x * 48 + lane;
    f[0] = (f[128] + f[224]) + (f[320] + f[416]);
  }
}
__device__ __forceinline__ void role_aux(const Params &p, LAS unsigned char *lds, rsrc_t rs, int x, int lane, const Early &e) {
  const int bid = BID, xg = bid & 7, idx = bid >> 3;
  const int j = idx & 7, j3 = idx & 15;
  const int h4 = x >> 1, o4 = x & 1;
  const int s12 = (j * 20 + h4 * 10) * 5120, s3 = OFF_WT3 + j3 * 120 * 1024, s4 = OFF_WT4 + j * 48 * 5120, lv = lane * 16;
  const int rot10 = 5 * o4 + ((j * 5) >> 2), rot2 = 5 * o4 + ((j & 1) * 5), rot20 = 5 * x + ((j3 & 3) * 5), rot24 = 3 * j;
#define W4SIG(t) ((t) == 0 ? 24 + x : 6 * x + (t) - 1)
#define W4KS(t) ((t) < 7 ? (W4SIG(t) < 14 ? rotk(W4SIG(t), rot24, 24) : 24 + rotk(W4SIG(t) - 14, rot24, 24)) \
                         : 24 * h4 + rotk((t) < 9 ? 14 + 2 * o4 + (t) - 7 : 18 + 3 * o4 + (t) - 9, rot24, 24))
#define LD1(n) ld16(rs, ((n) % 5 < 4 ? lv : lv4) + ((n) % 5 & 3) * 1024, OFF_WT1 + s12 + rotk((n) / 5, rot10, 10) * 5120)
#define LD2(n) ld16(rs, ((n) % 5 < 4 ? lv : lv4) + ((n) % 5 & 3) * 1024, OFF_WT2 + s12 + rotk((n) / 5, rot2, 10) * 5120)
#define LD3(n) ld16(rs, ((n) % 6 < 4 ? lv : lv4) + ((n) % 6 & 3) * 1024, k3[(n) / 6])
#define LD4(n) ld16(rs, ((n) % 5 < 4 ? lv : lv4) + ((n) % 5 & 3) * 1024, k4[(n) / 5])
#define STF(base, n, v) *(LAS v4u *)((base) + (n) * 1024) = (v)
  v4u A[25], B[25];
  u64 tag = 0;
  v2u wq0 = {0u, 0u};
  int wqo0;
  {
    const int l5 = min(lane & 31, 24), u0 = (lane < 32 ? rot10 : rot2) * 5 + l5;
    wqo0 = F_W + ((lane < 32 ? 0 : 100) + 50 * h4 + (u0 >= 50 ? u0 - 50 : u0)) * 64;
  }
  if (idx < 20) {
    const int c4 = xg * 20 + idx, th = x * 64 + lane;
    const int r16 = lane & 15, g = lane >> 4;
    const v2u ep = e.ep;
    v2u dv = ((const GAS v2u *)p.Dsk)[c4];
    const int xo = th * 8 * (D * 2) + c4 * 8;
    v2u xv[8];
#pragma unroll
    for (int i = 0; i < 4; ++i) xv[i] = e.l[2 + i];
    v4u sv[4];
    SCHED();
    {
      unsigned gn[8];
      f32x4 k0 = {0.f, 0.f, 0.f, 0.f};
      v4u t0[4], tb[4];
#pragma unroll
      for (int s = 0; s < 4; ++s) { UNDEF(t0[s]); UNDEF(tb[s]); }
      int oc = x * KT_SH, on = oc + 4 * KT_SH;
      const int mp = KT_PV - KT_PO + x * KT_PW;
      LAS unsigned char *trd = lds + r16 * TP + g * 16;
      SCHED();
      const float dt = bflo(pk2(__expf((x & 1) ? bfhi(e.ldt) : bflo(e.ldt)), 0.f));
      float ar[3], ai[3];
      get3(e.l[0], lane, ar); get3(e.l[1], lane, ai);
      const f32x2 pm0 = {dt * ar[0] * 1.4426950408889634f, dt * ai[0] * 0.15915494309189535f};
      f32x2 pm1 = {dt * ar[1] * 1.4426950408889634f, dt * ai[1] * 0.15915494309189535f};
      const f32x2 pm2 = {dt * ar[2] * 1.4426950408889634f, dt * ai[2] * 0.15915494309189535f};
#define GENS(e1, e128)                                                                                                 \
  {                                                                                                                    \
    f32x2 a2[2], a4[4];                                                                                                \
    cmulN<2>(a2, {e1, e128}, {e1, e128});                                                             \
    cmulN<4>(a4, {a2[0], a2[0], a2[1], a2[1]}, {e1, a2[0], e128, a2[1]});               \
    gn[0] = ph(e1); gn[1] = ph(a2[0]); gn[2] = ph(a4[0]); gn[3] = ph(a4[1]);                                           \
    gn[4] = ph(e128); gn[5] = ph(a2[1]); gn[6] = ph(a4[2]); gn[7] = ph(a4[3]);                                         \
    _Pragma("unroll") for (int i = 0; i < 8; ++i) PIN(gn[i]);                                                          \
  }
      {
        const f32x2 e1 = cexp_l(pm0.x, pm0.y, 1.f), e128 = cexp_l(pm0.x, pm0.y, 128.f);
        SCHED();
        GENS(e1, e128)
      }
#pragma unroll
      for (int i = 4; i < 8; ++i) UNDEF(xv[i]);
#pragma unroll
      for (int k = 0; k < 4; ++k) UNDEF(sv[k]);
      const v4u xd = {(unsigned)(size_t)p.x, (unsigned)((size_t)p.x >> 32) & 0xffffu, (unsigned)(L * D * 2), 0x00020000u};
      const v4u wd = {(unsigned)(size_t)p.ws, (unsigned)((size_t)p.ws >> 32) & 0xffffu, (unsigned)WS_BYTES, 0x00020000u};
      const int xo4 = xo + 4 * (D * 2), so = th * 64;
      SCHED();
#pragma unroll 1
      for (int r = 0; r < 3; ++r) {
        unsigned vb[32];
#define OUT0(a, b, c, d) TW4(mp, KT_PO, a, b, c, d, vb[a], vb[b], vb[c], vb[d]);
#define OUT1(a, b, c, d) TW4(oc, 0, a, b, c, d, vb[a], vb[b], vb[c], vb[d]);
        vb[0] = 0x00003c00u; vb[1] = gn[0]; vb[2] = gn[1]; vb[3] = gn[2]; vb[4] = gn[3];
        vb[8] = gn[4]; vb[16] = gn[5]; vb[20] = gn[6]; vb[24] = gn[7];
        OUT0(0, 1, 2, 3)
        unsigned b15[15], b8[8];
        hmulN<15>(b15, {vb[4], vb[4], vb[4], vb[24], vb[24], vb[24], vb[8], vb[8], vb[8], vb[8], vb[16], vb[16], vb[20], vb[20], vb[24]},
                  {vb[1], vb[2], vb[3], vb[8], vb[16], vb[20], vb[1], vb[2], vb[3], vb[4], vb[2], vb[4], vb[2], vb[4], vb[4]});
        vb[5] = b15[0]; vb[6] = b15[1]; vb[7] = b15[2]; vb[26] = b15[3]; vb[28] = b15[4]; vb[30] = b15[5];
        vb[9] = b15[6]; vb[10] = b15[7]; vb[11] = b15[8]; vb[12] = b15[9]; vb[17] = b15[10]; vb[18] = b15[11];
        vb[21] = b15[12]; vb[22] = b15[13]; vb[25] = b15[14];
        TW8(mp, KT_PO, vb, 4, 5, 6, 7, 8, 9, 10, 11, 0);
        TW8(oc, 0, vb, 16, 17, 18, 20, 21, 22, 24, 25, 0);
        hmulN<8>(b8, {vb[8], vb[8], vb[8], vb[16], vb[20], vb[26], vb[28], vb[30]}, {vb[5], vb[6], vb[7], vb[6], vb[6], vb[4], vb[4], vb[4]});
        vb[13] = b8[0]; vb[14] = b8[1]; vb[15] = b8[2]; vb[19] = b8[3]; vb[23] = b8[4]; vb[27] = b8[5]; vb[29] = b8[6]; vb[31] = b8[7];
        TW8(oc, 0, vb, 19, 23, 26, 27, 28, 29, 30, 31, 0);
        OUT0(12, 13, 14, 15)
#undef OUT0
#undef OUT1
        SCHED();
        if (r > 0) {
#pragma unroll
          for (int s = 0; s < 4; ++s) k0 = mfma_h(t0[s], tb[s], k0);
        }
        if (r < 2) {
          const f32x2 e1 = cexp_l(pm1.x, pm1.y, 1.f), e128 = cexp_l(pm1.x, pm1.y, 128.f);
          pm1 = pm2;
          SCHED();
          GENS(e1, e128)
        }
        KEEP8(vb); KEEP8((vb + 8)); KEEP8((vb + 16)); KEEP8((vb + 24));
        PIN(k0);
        asm volatile(
            "s_cmp_lg_u32 %12, 0\n\ts_cbranch_scc1 1f\n\t"
            "buffer_load_dwordx2 %0, %8, %10, 0 offen\n\tbuffer_load_dwordx2 %1, %8, %10, 0 offen offset:1280\n"
            "1:\n\ts_cmp_lg_u32 %12, 1\n\ts_cbranch_scc1 2f\n\t"
            "buffer_load_dwordx2 %2, %8, %10, 0 offen offset:2560\n\tbuffer_load_dwordx2 %3, %8, %10, 0 offen offset:3840\n"
            "2:\n\ts_cmp_lg_u32 %12, 2\n\ts_cbranch_scc1 3f\n\t"
            "buffer_load_dwordx4 %4, %9, %11, 0 offen offset:1024 sc1\n\tbuffer_load_dwordx4 %5, %9, %11, 0 offen offset:1040 sc1\n\t"
            "buffer_load_dwordx4 %6, %9, %11, 0 offen offset:1056 sc1\n\tbuffer_load_dwordx4 %7, %9, %11, 0 offen offset:1072 sc1\n"
            "3:"
            : "+v"(xv[4]), "+v"(xv[5]), "+v"(xv[6]), "+v"(xv[7]), "+v"(sv[0]), "+v"(sv[1]), "+v"(sv[2]), "+v"(sv[3])
            : "v"(xo4), "v"(so), "s"(xd), "s"(wd), "s"(r) : "scc", "memory");
        BARRIER();
#pragma unroll
        for (int s = 0; s < 4; ++s) {
          tb[s] = *(LAS v4u *)(trd + oc + s * 64);
          t0[s] = *(LAS v4u *)(trd + mp + KT_PO + s * 64);
        }
        const int t_ = oc; oc = on; on = t_;
        SCHED();
      }
#undef GENS
      const float kinv = *(LAS float *)(lds + L_TAG + 16 + x * 4);
      tag = uni64(ep) + 1ull;
      if (x == 0 && lane == 0) *(LAS v2u *)(lds + L_TAG) = split64(tag);
      SCHED();
#pragma unroll
      for (int s = 0; s < 4; ++s) k0 = mfma_h(t0[s], tb[s], k0);
      k0 *= kinv;
      LAS unsigned char *cmp = lds + CV_CMP + x * CV_CMPW + 16;
      *(LAS v2u *)(cmp + (128 * (g >> 1) + 4 * (g & 1) + 8 * r16) * 2) = v2u{pk2(k0.x, k0.y), pk2(k0.z, k0.w)};
    }
    BARRIER();
    static_assert(F_ST == 1024, "offsets in the asm above");
    asm volatile("s_waitcnt vmcnt(0)" : "+v"(xv[4]), "+v"(xv[5]), "+v"(xv[6]), "+v"(xv[7]), "+v"(sv[0]), "+v"(sv[1]), "+v"(sv[2]), "+v"(sv[3])::"memory");
    {
      const unsigned tl = (unsigned)tag;
      bool ok = ((sv[0].x ^ tl) | (sv[0].z ^ tl) | (sv[1].x ^ tl) | (sv[1].z ^ tl) | (sv[2].x ^ tl) | (sv[2].z ^ tl) | (sv[3].x ^ tl) | (sv[3].z ^ tl)) == 0u;
      if (__builtin_expect(!__all(ok), 0)) {
#pragma unroll 1
        for (unsigned spins = 0; spins < SPIN_CAP; ++spins) {
#pragma unroll
          for (int k = 0; k < 4; ++k) sv[k] = ld16(rs, th * 64 + k * 16, F_ST);
          ok = true;
#pragma unroll
          for (int k = 0; k < 4; ++k) ok = ok && sv[k].x == (unsigned)tag && sv[k].z == (unsigned)tag;
          if (__all(ok)) break;
          __builtin_amdgcn_s_sleep(2);
        }
      }
    }
    PIN(dv);
#pragma unroll
    for (int i = 0; i < 4; ++i) PIN(xv[i]);
    wq0 = ld8(rs, wqo0, 0);
    SCHED();
    const f32x2 d0 = {bflo(dv.x), bfhi(dv.x)}, d1 = {bflo(dv.y), bfhi(dv.y)};
    f32x2 u0[8], u1[8], e0[8], e1[8];
#pragma unroll
    for (int i = 0; i < 8; ++i) {
      const unsigned pl = (i & 1) ? sv[i >> 1].w : sv[i >> 1].y;
      const float r = bfhi(pl), nm = bflo(pl);
      const f32x2 x0 = {bflo(xv[i].x), bfhi(xv[i].x)}, x1 = {bflo(xv[i].y), bfhi(xv[i].y)};
      u0[i] = __builtin_elementwise_fma(x0, f32x2{r, r}, f32x2{nm, nm});
      u1[i] = __builtin_elementwise_fma(x1, f32x2{r, r}, f32x2{nm, nm});
      e0[i] = __builtin_elementwise_fma(u0[i], d0, x0);
      e1[i] = __builtin_elementwise_fma(u1[i], d1, x1);
    }
    const int ub = CV_UE + 4096 + th * 128 + ((th << 3) & 0x70);
    v4u uq[8];
#define UE_PUT(slot0, A, c) *(LAS v4u *)(lds + (ub ^ ((slot0) * 16))) = uq[slot0] = v4u{pk2(A[0].c, A[1].c), pk2(A[2].c, A[3].c), pk2(A[4].c, A[5].c), pk2(A[6].c, A[7].c)}
    UE_PUT(0, u0, x); UE_PUT(1, u0, y); UE_PUT(2, u1, x); UE_PUT(3, u1, y);
    UE_PUT(4, e0, x); UE_PUT(5, e0, y); UE_PUT(6, e1, x); UE_PUT(7, e1, y);
    KEEP8(uq);
#undef UE_PUT
    BARRIER();
  } else {
    BARRIER();
    tag = uni64(*(LAS v2u *)(lds + L_TAG));
    wq0 = split64(~tag);
  }
  wait_done(rs, wqo0, wq0, tag);
  const v2u fini = ld8(rs, F_INIT + (lane & 31) * 64, 0);
  int lv4 = lv + 4096;
  PIN(lv4);
#pragma unroll
  for (int i = 0; i < 25; ++i) A[i] = LD1(i);
#pragma unroll
  for (int i = 0; i < 25; ++i) B[i] = LD2(i);
  SCHED();
  if (x == 0) wait_done(rs, F_INIT + (lane & 31) * 64, fini, tag);
  BARRIER();
  BARRIER();
  const v4u ones = {ONE2, ONE2, ONE2, ONE2};
  {
    f32x4 c5[5];
#pragma unroll
    for (int i = 0; i < 5; ++i) c5[i] = f32x4{0.f, 0.f, 0.f, 0.f};
    LAS unsigned char *o = lds + x * 25 * 1024 + lv;
#pragma unroll
    for (int i = 0; i < 25; ++i) { STF(o, i, A[i]); c5[i % 5] = mfma(A[i], ones, c5[i % 5]); SCHED(); }
    colsum_part<5>(lds, x, lane, c5);
  }
  int wqo1;
  {
    int ln = lane;
    PIN(ln);
    const int t = (ln & 15) >> 1, t5 = t < 5 ? t : 0, c5 = (80 * j + 79 * (ln & 1)) >> 7, c6 = (96 * j3 + 95 * (ln & 1)) >> 7;
    const int t4 = ln >= 16 && ln < 40 ? (ln - 16) >> 1 : 0;
    wqo1 = F_W + (ln < 16 ? 200 + rotk(t5, rot20, 20) * 12 + c6 : 440 + W4KS(t4) * 5 + c5) * 64;
  }
  const v2u wq = ld8(rs, wqo1, 0);
  int k3[5], k4[12];
#pragma unroll
  for (int t = 0; t < 5; ++t) { k3[t] = s3 + rotk(t, rot20, 20) * 6144; asm volatile("" : "+s"(k3[t])); }
#pragma unroll
  for (int t = 0; t < 12; ++t) { k4[t] = s4 + W4KS(t) * 5120; asm volatile("" : "+s"(k4[t])); }
  BARRIER();
  colsum_add(lds, x, lane);
  wait_done(rs, wqo1, wq, tag);
  BARRIER();
#pragma unroll
  for (int i = 0; i < 20; ++i) A[i] = LD3(i);
  BARRIER();
#pragma unroll
  for (int i = 20; i < 25; ++i) A[i] = LD3(i);
  BARRIER();
  {
    LAS unsigned char *o = lds + x * 25 * 1024 + lv;
#pragma unroll
    for (int i = 0; i < 25; ++i) { STF(o, i, B[i]); if (i < 5) B[i] = LD3(25 + i); SCHED(); }
  }
  BARRIER();
  BARRIER();
#pragma unroll
  for (int i = 5; i < 25; ++i) { B[i] = LD4(i - 5); SCHED(); }
  BARRIER();
  BARRIER();
  {
    f32x4 c6[6];
#pragma unroll
    for (int i = 0; i < 6; ++i) c6[i] = f32x4{0.f, 0.f, 0.f, 0.f};
    LAS unsigned char *o = lds + x * 30 * 1024 + lv;
#pragma unroll
    for (int i = 0; i < 25; ++i) { STF(o, i, A[i]); c6[i % 6] = mfma(A[i], ones, c6[i % 6]); SCHED(); }
#pragma unroll
    for (int i = 0; i < 5; ++i) { STF(o, 25 + i, B[i]); c6[(25 + i) % 6] = mfma(B[i], ones, c6[(25 + i) % 6]); }
    colsum_part<6>(lds, x, lane, c6);
#pragma unroll
    for (int i = 0; i < 5; ++i) STF(lds + (24 + x) * 5120 + lv, i, B[5 + i]);
  }
  BARRIER();
  colsum_add(lds, x, lane);
  BARRIER();
  v4u C[5];
#define LDQ(i) do { if ((i) < 25) A[(i) < 25 ? (i) : 0] = LD4(20 + (i)); else if ((i) < 35) B[(i) < 35 && (i) >= 25 ? (i) - 25 : 0] = LD4(20 + (i)); else C[(i) >= 35 ? (i) - 35 : 0] = LD4(20 + (i)); } while (0)
#pragma unroll
  for (int i = 0; i < 40; ++i) { LDQ(i); __builtin_amdgcn_s_sleep(1); SCHED(); }
  BARRIER();
  {
    LAS unsigned char *o = lds + 6 * x * 5120 + lv;
#pragma unroll
    for (int i = 0; i < 15; ++i) STF(o, i, B[10 + i]);
#pragma unroll
    for (int i = 0; i < 15; ++i) STF(o, 15 + i, A[i]);
  }
  BARRIER();
  BARRIER();
  {
    LAS unsigned char *o = lds + (h4 * 14 + 2 * o4) * 5120 + lv;
#pragma unroll
    for (int i = 0; i < 10; ++i) STF(o, i, A[15 + i]);
  }
  BARRIER();
  {
    LAS unsigned char *o = lds + (h4 * 14 + 4 + 3 * o4) * 5120 + lv;
#pragma unroll
    for (int i = 0; i < 10; ++i) STF(o, i, B[i]);
#pragma unroll
    for (int i = 0; i < 5; ++i) STF(o, 10 + i, C[i]);
  }
  BARRIER();
  BARRIER();
}

__device__ void code_end();
__global__ void __launch_bounds__(512, 2) k_fused(Params p) {
  __shared__ __attribute__((aligned(16))) unsigned char smem[LDS_BYTES];
  LAS unsigned char *lds = (LAS unsigned char *)smem;
  const int lane = threadIdx.x & 63, w = __builtin_amdgcn_readfirstlane(threadIdx.x >> 6);
  unsigned cpf = 0;
  {
    u64 pc0;
    asm volatile("s_getpc_b64 %0" : "=s"(pc0));
    const unsigned o = (((BID >> 3) - 20) * 64 + lane) * 128;
    const long long len = (long long)(u64)&code_end - (long long)pc0;
    if (w == 3 && BID >= 160 && (long long)o < len) cpf = *(const GAS unsigned *)((const GAS unsigned char *)(pc0 & ~127ull) + o);
    SCHED();
  }
  const rsrc_t rs = __builtin_amdgcn_make_buffer_rsrc((void *)p.ws, 0, WS_BYTES, 0x00020000);
  Early e;
  {
    const int idx = BID >> 3, d = (((BID & 7) * 20 + (idx < 20 ? idx : 0)) * 4 + (w & 3));
    e.l[0] = ld3(p.Are, d, lane); e.l[1] = ld3(p.Aim, d, lane);
    {
      int z = (d & ~1) * 2;
      PIN(z);
      e.ldt = *(const GAS unsigned *)((const GAS unsigned char *)p.logdt + (unsigned)z);
    }
    e.ep = ld8(rs, 0, 0);
    const bool xr = w >= 4 && idx < 20;
    const unsigned vo = xr ? (threadIdx.x - 256) * 8 * (D * 2) : (6 * lane) & ~3;
    const GAS unsigned char *const src[4] = {(const GAS unsigned char *)p.Bre, (const GAS unsigned char *)p.Bim, (const GAS unsigned char *)p.Cre,
                                             (const GAS unsigned char *)p.Cim};
    SCHED();
    if (xr) __builtin_amdgcn_s_sleep(4);
    SCHED();
#pragma unroll
    for (int i = 0; i < 4; ++i) {
      const GAS unsigned char *b = xr ? (const GAS unsigned char *)p.x + i * (D * 2) + (d >> 2) * 8 : src[i] + d * (NM * 2);
      e.l[2 + i] = *(const GAS v2u_a4 *)(b + vo);
    }
    asm volatile("" ::: "memory");
    SCHED();
  }
  if (BID >= 160) {
    const int n = BID - 160, u0 = ((w + 1) % 5) * 96 + n, grp = n + 96 * w;
    const u16 *w1 = p.w1, *w2 = p.w2, *w3 = p.w3, *w4 = p.w4;
    asm volatile("" : "+s"(w1), "+s"(w2), "+s"(w3), "+s"(w4));
    v4u t[8], t8, t9;
    {
      const GAS unsigned char *xr = (const GAS unsigned char *)p.x + (grp & 255) * 8 * (D * 2);
      const unsigned vo = (lane >> 3) * (D * 2) + (lane & 7) * 16;
      t8 = *(const GAS v4u *)(xr + 1024 + vo); t9 = *(const GAS v4u *)(xr + 1152 + vo);
      retile_load(w1, w2, w3, w4, u0, lane, t, w < 3 ? xr : nullptr);
    }
    SCHED();
    int far = 1;
    asm volatile("" : "+s"(far));
    if (__builtin_expect(far, 0)) {
      if (w == 0 && lane == 0) *(LAS v2u *)(lds + L_TAG) = split64(uni64(e.ep) + 1ull);
      BARRIER();
      const u64 tag = uni64(*(LAS v2u *)(lds + L_TAG));
      if (w >= 3) {
#pragma unroll 1
        for (int unit = u0; unit < 680; unit += 480) {
          if (unit >= 480) retile_load(w1, w2, w3, w4, unit, lane, t);
          retile_store(lds + (w - 3) * 8 * FP, rs, unit, lane, t);
          DRAIN();
          if (lane == 0) st8(rs, F_W + unit * 64, 0, split64(tag));
        }
        asm volatile("" ::"v"(cpf));
      } else if (grp < 256) {
        float s0 = 0.f, q0 = 0.f, s1 = 0.f, q1 = 0.f;
#pragma unroll
        for (int i = 0; i < 10; ++i) {
          const v4u v = i < 8 ? t[i < 8 ? i : 0] : i == 8 ? t8 : t9;
          s0 = dot2(v.x, ONE2, s0); q0 = dot2(v.x, v.x, q0); s1 = dot2(v.y, ONE2, s1); q1 = dot2(v.y, v.y, q1);
          s0 = dot2(v.z, ONE2, s0); q0 = dot2(v.z, v.z, q0); s1 = dot2(v.w, ONE2, s1); q1 = dot2(v.w, v.w, q1);
        }
        s0 += s1; q0 += q1;
        s0 = dpp_add<0x141>(dpp_add<0x4e>(dpp_add<0xb1>(s0)));
        q0 = dpp_add<0x141>(dpp_add<0x4e>(dpp_add<0xb1>(q0)));
        const float mean = s0 * (1.f / D);
        const float var = fmaxf(q0 * (1.f / D) - mean * mean, 0.f);
        const float rsd = bflo(pk2(__builtin_amdgcn_rsqf(var + EPS), 0.f));
        if ((lane & 7) == 0) st8(rs, F_ST + grp * 64 + lane, 0, v2u{(unsigned)tag, pk2(-mean * rsd, rsd)});
      } else {
        const v2u c0 = split64(tag << 8);
        const v4u cv = {c0.x, c0.y, c0.x, c0.y};
        st16(rs, lane * 16, C_S1 + (n - 64) * 1024, cv);
        if (n - 64 < NCNT / 16 - 32) st16(rs, lane * 16, C_S1 + (n - 32) * 1024, cv);
        DRAIN();
        if (lane == 0) st8(rs, F_INIT + (n - 64) * 64, 0, split64(tag));
      }
    }
    __builtin_amdgcn_s_waitcnt(0x0F70);
  }
  int w2 = w;
  asm volatile("" : "+s"(w2));
  if (__builtin_expect(w < 4, 1)) {
    role_compute(p, lds, rs, w, lane, e);
#pragma unroll
    for (int i = 0; i < 6; ++i) UNDEF(e.l[i]);
    UNDEF(e.ep); UNDEF(e.ldt);
  }
  if (w2 >= 4) role_aux(p, lds, rs, w - 4, lane, e);
}

__device__ __attribute__((noinline, used)) void code_end() { asm volatile(""); }

extern "C" void kernel_launch(void *const *d_in, const int *in_sizes, int n_in, void *d_out, int out_size, void *d_ws,
                              size_t ws_size, hipStream_t stream) {
  Params p;
  p.x = (const u16 *)d_in[0]; p.Are = (const u16 *)d_in[1]; p.Aim = (const u16 *)d_in[2]; p.Bre = (const u16 *)d_in[3];
  p.Bim = (const u16 *)d_in[4]; p.Cre = (const u16 *)d_in[5]; p.Cim = (const u16 *)d_in[6]; p.logdt = (const u16 *)d_in[7];
  p.Dsk = (const u16 *)d_in[8]; p.w1 = (const u16 *)d_in[10]; p.w2 = (const u16 *)d_in[11]; p.w3 = (const u16 *)d_in[12];
  p.w4 = (const u16 *)d_in[13]; p.out = (u16 *)d_out; p.ws = (unsigned char *)d_ws;
  k_fused<<<256, 512, 0, stream>>>(p);
}
```
